```python
import torch

def _softmax(x, axis=-1):
    return torch.softmax(x, dim=axis)
def _gelu(x):
    return torch.nn.functional.gelu(x, approximate="tanh")
def _silu(x):
    return torch.nn.functional.silu(x)
def _sigmoid(x):
    return torch.sigmoid(x)
def _layernorm(x, axis=-1, eps=1e-5):
    m = x.mean(dim=axis, keepdim=True)
    v = x.var(dim=axis, keepdim=True, unbiased=False)
    return (x - m) / torch.sqrt(v + eps)
def _scatter_add(tgt, idx, src, axis=0):
    return tgt.clone().index_add_(axis, idx, src)
def _segment_sum(src, seg, n):
    out = torch.zeros((n,) + src.shape[1:], dtype=src.dtype)
    return out.index_add_(0, seg, src)
def _pad(x, pw):
    flat = tuple(v for pair in reversed(pw) for v in pair)
    return torch.nn.functional.pad(x, flat)

kernel_name = 'parallel_sum__ssm_conv_h3__glu_gated_geglu__prompt'

def setup_inputs():

    _w = (lambda a: (lambda t: t.to(torch.bfloat16) if t.is_floating_point() else t)(torch.as_tensor(a)))
    def _A_setup():
        B, S, D = 1, 2048, 640
        ks = 4
        return {
            "q": _w((lambda *s: torch.randn(*s, dtype=torch.bfloat16) * 0.05)(B, S, D)),
            "k": _w((lambda *s: torch.randn(*s, dtype=torch.bfloat16) * 0.2)(B, S, D)),
            "v": _w((lambda *s: torch.randn(*s, dtype=torch.bfloat16) * 0.2)(B, S, D)),
            "shift_k": _w((lambda *s: torch.randn(*s, dtype=torch.bfloat16) * 0.2)(D, ks) * 0.5),
            "ssm_k": _w((lambda *s: torch.randn(*s, dtype=torch.bfloat16) * 0.4)(D, S) * (1.0 / S ** 0.5)),
            "ssm_D": _w((lambda *s: torch.randn(*s, dtype=torch.bfloat16) * 0.2)(D)),
        }
    _A_in = _A_setup()

    def _B_setup():
        B, S, Dm, Df = 1, 2048, 640, 1728
        return {
            "x": _w((lambda *s: torch.randn(*s, dtype=torch.bfloat16) * 0.05)(B, S, Dm)),
            "w_gate": _w((lambda *s: torch.randn(*s, dtype=torch.bfloat16) * 0.4)(Dm, Df) * (Dm ** -0.5)),
            "w_up": _w((lambda *s: torch.randn(*s, dtype=torch.bfloat16) * 0.4)(Dm, Df) * (Dm ** -0.5)),
            "w_down": _w((lambda *s: torch.randn(*s, dtype=torch.bfloat16) * 0.4)(Df, Dm) * (Df ** -0.5)),
        }
    _B_in = _B_setup()

    return {
        "A_q": _A_in["q"],
        "A_k": _A_in["k"],
        "A_v": _A_in["v"],
        "A_shift_k": _A_in["shift_k"],
        "A_ssm_k": _A_in["ssm_k"],
        "A_ssm_D": _A_in["ssm_D"],
        "B_x": _B_in["x"],
        "B_w_gate": _B_in["w_gate"],
        "B_w_up": _B_in["w_up"],
        "B_w_down": _B_in["w_down"],
    }

def reference(A_q, A_k, A_v, A_shift_k, A_ssm_k, A_ssm_D, B_x, B_w_gate, B_w_up, B_w_down):
    def _A_ref(q, k, v, shift_k, ssm_k, ssm_D):
        B, S, D = q.shape
        ks = shift_k.shape[-1]
        k_pad = _pad(k, ((0, 0), (ks - 1, 0), (0, 0)))
        k_shift = torch.zeros_like(k)
        for t in range(ks):
            k_shift = k_shift + k_pad[:, t:t + S, :] * shift_k[:, ks - 1 - t][None, None, :]
        kv = k_shift * v
        L = 2 * S
        kv_pad = _pad(kv, ((0, 0), (0, S), (0, 0)))
        kf_pad = _pad(ssm_k, ((0, 0), (0, S)))
        kv_f = (lambda x, axes=(-2,-1): torch.fft.rfft2(x.float(), dim=axes))(kv_pad[:, None, :, :], axes=(1, 2))
        kf_f = (lambda x, axes=(-2,-1): torch.fft.rfft2(x.float(), dim=axes))(kf_pad[:, None, :], axes=(1, 2))
        kf_f = (lambda x, *d: x.permute(*d))(kf_f, 1, 2, 0)
        y_f = kv_f * kf_f[None, :, :, :]
        y = (lambda x, s, axes=(-2,-1): torch.fft.irfft2(x, s=s, dim=axes).to(torch.bfloat16))(y_f, (1, L), axes=(1, 2))[:, 0, :S, :]
        y = y + kv * ssm_D[None, None, :]
        return q * y

    def _B_ref(x, w_gate, w_up, w_down):
        g = torch.einsum("bsd,df->bsf", x, w_gate)
        u = torch.einsum("bsd,df->bsf", x, w_up)
        h = _gelu(g) * u
        return torch.einsum("bsf,fd->bsd", h, w_down)

    x = A_q
    xn = _layernorm(x, axis=-1)
    return x + _A_ref(xn, A_k, A_v, A_shift_k, A_ssm_k, A_ssm_D) + _B_ref(xn, B_w_gate, B_w_up, B_w_down)
```

```cpp
#include <hip/hip_runtime.h>
#include <hip/hip_bf16.h>
#include <stdint.h>

typedef unsigned short u16;
static __device__ __forceinline__ float bf2f(u16 v) { return __uint_as_float(((unsigned)v) << 16); }
static __device__ __forceinline__ u16 f2bf(float f) {
    __hip_bfloat16 b = __float2bfloat16(f);
    return *reinterpret_cast<u16*>(&b);
}
static __device__ __forceinline__ float rbf(float f) { return bf2f(f2bf(f)); }

constexpr int S = 2048, D = 640, DF = 1728;

__global__ void ln_kernel(const u16* x, u16* xn) {
    int row = blockIdx.x;
    int lane = threadIdx.x;
    float v[10];
    float s = 0;
    for (int i = 0; i < 10; ++i) { v[i] = bf2f(x[row * D + lane + 64 * i]); s += v[i]; }
    for (int o = 32; o > 0; o >>= 1) s += __shfl_xor(s, o);
    float mean = s / D;
    float q = 0;
    for (int i = 0; i < 10; ++i) { float d = v[i] - mean; q += d * d; }
    for (int o = 32; o > 0; o >>= 1) q += __shfl_xor(q, o);
    float var = rbf(q / D);
    float m = rbf(mean);
    float den = rbf(sqrtf(rbf(var + 1e-5f)));
    for (int i = 0; i < 10; ++i) xn[row * D + lane + 64 * i] = f2bf(rbf(v[i] - m) / den);
}

__global__ void kv_kernel(const u16* k, const u16* v, const u16* sk, u16* kvT) {
    int idx = blockIdx.x * blockDim.x + threadIdx.x;
    if (idx >= S * D) return;
    int s = idx / D, d = idx % D;
    float acc = 0;
    for (int t = 0; t < 4; ++t) {
        int ss = s + t - 3;
        float kk = ss >= 0 ? bf2f(k[ss * D + d]) : 0.f;
        acc = rbf(acc + rbf(kk * bf2f(sk[d * 4 + 3 - t])));
    }
    kvT[d * S + s] = f2bf(acc * bf2f(v[idx]));
}

__global__ void conv_kernel(const u16* kvT, const u16* h, const u16* Dv, u16* yT) {
    __shared__ float su[S], sh[S];
    int d = blockIdx.x;
    for (int i = threadIdx.x; i < S; i += blockDim.x) { su[i] = bf2f(kvT[d * S + i]); sh[i] = bf2f(h[d * S + i]); }
    __syncthreads();
    for (int s = threadIdx.x; s < S; s += blockDim.x) {
        float acc = 0;
        for (int j = 0; j <= s; ++j) acc += sh[j] * su[s - j];
        float y = rbf(acc);
        y = rbf(y + rbf(su[s] * bf2f(Dv[d])));
        yT[d * S + s] = f2bf(y);
    }
}

__global__ void gemm1_kernel(const u16* xn, const u16* wg, const u16* wu, u16* h) {
    __shared__ float sx[16][D];
    int r0 = blockIdx.y * 16, c = blockIdx.x * 64 + (threadIdx.x & 63);
    for (int i = threadIdx.x; i < 16 * D; i += blockDim.x) sx[i / D][i % D] = bf2f(xn[(r0 + i / D) * D + i % D]);
    __syncthreads();
    int rq = threadIdx.x >> 6;
    float g[4] = {0, 0, 0, 0}, u[4] = {0, 0, 0, 0};
    for (int k = 0; k < D; ++k) {
        float a = bf2f(wg[k * DF + c]), b = bf2f(wu[k * DF + c]);
        for (int r = 0; r < 4; ++r) { float xv = sx[rq * 4 + r][k]; g[r] += xv * a; u[r] += xv * b; }
    }
    for (int r = 0; r < 4; ++r) {
        float gg = rbf(g[r]), uu = rbf(u[r]);
        float z = 0.7978845608028654f * (gg + 0.044715f * gg * gg * gg);
        float ge = rbf(0.5f * gg * (1.f + tanhf(z)));
        h[(r0 + rq * 4 + r) * DF + c] = f2bf(ge * uu);
    }
}

__global__ void gemm2_kernel(const u16* h, const u16* wd, const u16* x, const u16* xn, const u16* yT, u16* out) {
    __shared__ float sx[8][DF];
    int r0 = blockIdx.y * 8, c = blockIdx.x * 64 + (threadIdx.x & 63);
    for (int i = threadIdx.x; i < 8 * DF; i += blockDim.x) sx[i / DF][i % DF] = bf2f(h[(r0 + i / DF) * DF + i % DF]);
    __syncthreads();
    int rq = threadIdx.x >> 6;
    float a[2] = {0, 0};
    for (int k = 0; k < DF; ++k) {
        float w = bf2f(wd[k * D + c]);
        for (int r = 0; r < 2; ++r) a[r] += sx[rq * 2 + r][k] * w;
    }
    for (int r = 0; r < 2; ++r) {
        int row = r0 + rq * 2 + r;
        float bo = rbf(a[r]);
        float ao = rbf(bf2f(xn[row * D + c]) * bf2f(yT[c * S + row]));
        float o = rbf(bf2f(x[row * D + c]) + ao);
        out[row * D + c] = f2bf(o + bo);
    }
}

extern "C" void kernel_launch(void* const* d_in, const int* in_sizes, int n_in,
                              void* d_out, int out_size, void* d_ws, size_t ws_size,
                              hipStream_t stream) {
    const u16* q = (const u16*)d_in[0];
    const u16* k = (const u16*)d_in[1];
    const u16* v = (const u16*)d_in[2];
    const u16* sk = (const u16*)d_in[3];
    const u16* ssmk = (const u16*)d_in[4];
    const u16* ssmD = (const u16*)d_in[5];
    const u16* wg = (const u16*)d_in[7];
    const u16* wu = (const u16*)d_in[8];
    const u16* wd = (const u16*)d_in[9];
    u16* out = (u16*)d_out;
    char* ws = (char*)d_ws;
    u16* xn = (u16*)ws;
    u16* kvT = (u16*)(ws + (4 << 20));
    u16* yT = (u16*)(ws + (8 << 20));
    u16* h = (u16*)(ws + (12 << 20));
    ln_kernel<<<S, 64, 0, stream>>>(q, xn);
    kv_kernel<<<(S * D + 255) / 256, 256, 0, stream>>>(k, v, sk, kvT);
    conv_kernel<<<D, 256, 0, stream>>>(kvT, ssmk, ssmD, yT);
    gemm1_kernel<<<dim3(DF / 64, S / 16), 256, 0, stream>>>(xn, wg, wu, h);
    gemm2_kernel<<<dim3(D / 64, S / 8), 256, 0, stream>>>(h, wd, q, xn, yT, out);
}
```

```cpp
#include <hip/hip_runtime.h>
#include <stdint.h>
typedef unsigned short u16;
typedef unsigned int u32;
typedef __attribute__((ext_vector_type(8))) __bf16 bf16x8;
typedef __attribute__((ext_vector_type(4))) __bf16 bf16x4;
typedef __attribute__((ext_vector_type(2))) __bf16 bf16x2;
typedef __attribute__((ext_vector_type(4))) float f32x4;
typedef __attribute__((ext_vector_type(2))) float f32x2;
typedef __attribute__((ext_vector_type(4))) unsigned u32x4;
typedef __attribute__((ext_vector_type(2))) unsigned u32x2;
typedef __amdgpu_buffer_rsrc_t rsrc_t;
#define AS3 __attribute__((address_space(3)))
#define DEV __device__ __forceinline__
constexpr int S = 2048, D = 640, DF = 1728;
constexpr u32 WS_F0 = 1024, WS_F1 = 2048, WS_F2 = 4096, WS_ST = 8192, WS_CS = 32768;
constexpr u32 WS_KV = 1u << 20, WS_Y = 4u << 20, WS_WD = 8u << 20, WS_H = 12u << 20, WS_SIZE = 20u << 20;
constexpr u32 XB = 30720, SLOT = XB + 16384, SP = 3 * SLOT;
constexpr u32 FLG = 163840 - 256;
constexpr u32 L_FULL = FLG, L_CNT0 = FLG + 8, L_P0CNT = FLG + 12, L_FREE = FLG + 16, L_P0ALL = FLG + 32, L_EPI = FLG + 36, L_IMG = FLG + 40,
              L_HCNT = FLG + 52, L_YCNT = FLG + 56, L_HRDY = FLG + 60, L_WDL = FLG + 64, L_BD = FLG + 80, L_AD = FLG + 84, L_YRDY = FLG + 88, L_TAG = FLG + 92;
constexpr u32 STG = SP;
constexpr u32 EPI = 148736, ECS = EPI + 3840, YST = 155392, YT = 153344, RB = 40960;
constexpr u32 IMG_U0 = 4160, IMG_U1 = 4160 + 5472, IMGSZ = 4160 + 5472 + 5440;
constexpr u32 LDS_BYTES = 163840;
constexpr u32 OOB = 0x7fffffffu;

DEV u32 rfl(u32 v) { return __builtin_amdgcn_readfirstlane(v); }
DEV rsrc_t mkrs(const void* p, u32 bytes) { return __builtin_amdgcn_make_buffer_rsrc((void*)p, 0, bytes, 0x00020000); }
DEV void dma16(u32 m0v, u32 voff, rsrc_t rs, u32 soff) {
    asm volatile("s_mov_b32 m0, %0\n\ts_nop 0\n\tbuffer_load_dwordx4 %1, %2, %3 offen lds" ::"s"(m0v), "v"(voff), "s"(rs), "s"(soff) : "memory");
}
DEV void dma16c(u32 m0v, u32 voff, rsrc_t rs, u32 soff) {
    asm volatile("s_mov_b32 m0, %0\n\ts_nop 0\n\tbuffer_load_dwordx4 %1, %2, %3 offen sc1 lds" ::"s"(m0v), "v"(voff), "s"(rs), "s"(soff) : "memory");
}
DEV void st128(u32x4 v, u32 voff, rsrc_t rs, u32 soff) {
    asm volatile("buffer_store_dwordx4 %0, %1, %2, %3 offen sc1\n\ts_nop 1" ::"v"(v), "v"(voff), "s"(rs), "s"(soff) : "memory");
}
DEV void st64(u32x2 v, u32 voff, rsrc_t rs, u32 soff) {
    asm volatile("buffer_store_dwordx2 %0, %1, %2, %3 offen sc1\n\ts_nop 1" ::"v"(v), "v"(voff), "s"(rs), "s"(soff) : "memory");
}
DEV void st32(u32 v, u32 voff, rsrc_t rs) { __builtin_amdgcn_raw_buffer_store_b32(v, rs, voff, 0, 16); }
DEV u32x4 ld128(rsrc_t rs, u32 voff, u32 soff) { return __builtin_amdgcn_raw_buffer_load_b128(rs, voff, soff, 0); }
DEV u32x4 ld128c(rsrc_t rs, u32 voff, u32 soff) { return __builtin_amdgcn_raw_buffer_load_b128(rs, voff, soff, 16); }
DEV u32x2 ld64(rsrc_t rs, u32 voff, u32 soff) { return __builtin_amdgcn_raw_buffer_load_b64(rs, voff, soff, 0); }
DEV u32x2 ld64c(rsrc_t rs, u32 voff, u32 soff) { return __builtin_amdgcn_raw_buffer_load_b64(rs, voff, soff, 16); }
DEV bf16x8 lds128(u32 a) { return *(const AS3 bf16x8*)(uintptr_t)a; }
DEV f32x4 lds128f(u32 a) { return *(const AS3 f32x4*)(uintptr_t)a; }
DEV u32x4 lds128u(u32 a) { return *(const AS3 u32x4*)(uintptr_t)a; }
DEV u32 lds32(u32 a) { return *(const AS3 u32*)(uintptr_t)a; }
DEV void sts128(u32 a, u32x4 v) { *(AS3 u32x4*)(uintptr_t)a = v; }
DEV void sts128f(u32 a, f32x4 v) { *(AS3 f32x4*)(uintptr_t)a = v; }
DEV void sts32(u32 a, u32 v) { *(AS3 u32*)(uintptr_t)a = v; }
DEV bf16x4 ldstr(u32 a) { return __builtin_amdgcn_ds_read_tr16_b64_v4bf16((AS3 bf16x4*)(uintptr_t)a); }
DEV u32x2 ldstru(u32 a) { return __builtin_bit_cast(u32x2, ldstr(a)); }
DEV bf16x8 cat(bf16x4 a, bf16x4 b) { return __builtin_shufflevector(a, b, 0, 1, 2, 3, 4, 5, 6, 7); }
DEV bf16x8 asbf(u32x4 v) { return __builtin_bit_cast(bf16x8, v); }
DEV u32 pk2(float a, float b) { f32x2 f = {a, b}; return __builtin_bit_cast(u32, __builtin_convertvector(f, bf16x2)); }
DEV float bfr(float a) { return __uint_as_float(pk2(a, 0.f) << 16); }
DEV float blo(u32 v) { return __uint_as_float(v << 16); }
DEV float bhi(u32 v) { return __uint_as_float(v & 0xffff0000u); }
DEV float dot2(u32 a, u32 b, float c) { return __builtin_amdgcn_fdot2_f32_bf16(__builtin_bit_cast(bf16x2, a), __builtin_bit_cast(bf16x2, b), c, false); }
#define WAITV(n) asm volatile("s_waitcnt vmcnt(" #n ")" ::: "memory")
#define WAITL() asm volatile("s_waitcnt lgkmcnt(0)" ::: "memory")
#define BARL() asm volatile("s_waitcnt lgkmcnt(0)\n\ts_barrier" ::: "memory")
#define MFMA16(a, b, c) __builtin_amdgcn_mfma_f32_16x16x32_bf16(a, b, c, 0, 0, 0)
DEV u32 ldsflag1(u32 a) { return *(volatile const AS3 u32*)(uintptr_t)a; }
DEV void ldsflagw(u32 a, u32 v) { *(volatile AS3 u32*)(uintptr_t)a = v; }
DEV u32 lds_add(u32 a) {
    u32 r, one = 1;
    asm volatile("s_waitcnt lgkmcnt(0)\n\ts_mov_b64 exec, 1\n\tds_add_rtn_u32 %0, %1, %2\n\ts_mov_b64 exec, -1\n\ts_waitcnt lgkmcnt(0)" : "=&v"(r) : "v"(a), "v"(one) : "memory");
    return rfl(r);
}
DEV void lds_wait_ge(u32 have, u32 need, u32 vaddr) {
    u32 vt, st, cn;
    asm volatile(
        "s_cmp_lt_u32 %3, %4\n\t"
        "s_cbranch_scc1 Lslow%=\n"
        "Ldone%=:\n\t"
        ".subsection 1\n"
        "Lslow%=:\n\t"
        "s_mov_b32 %2, 0x40000\n"
        "Lloop%=:\n\t"
        "ds_read_b32 %0, %5\n\t"
        "s_sub_u32 %2, %2, 1\n\t"
        "s_cbranch_scc1 Ldone%=\n\t"
        "s_waitcnt lgkmcnt(0)\n\t"
        "v_readfirstlane_b32 %1, %0\n\t"
        "s_cmp_lt_u32 %1, %4\n\t"
        "s_cbranch_scc1 Lloop%=\n\t"
        "s_branch Ldone%=\n\t"
        ".subsection 0\n"
        : "=&v"(vt), "=&s"(st), "=&s"(cn) : "s"(have), "s"(need), "v"(vaddr) : "memory", "scc");
}
DEV void lds_spin(u32 vaddr, u32 need) {
    u32 vt, cn;
    asm volatile(
        "s_mov_b32 %1, 0x40000\n"
        "Lsp%=:\n\t"
        "ds_read_b32 %0, %2\n\t"
        "s_waitcnt lgkmcnt(0)\n\t"
        "v_cmp_gt_u32 vcc, %3, %0\n\t"
        "s_cbranch_vccz Lse%=\n\t"
        "s_sleep 1\n\t"
        "s_sub_u32 %1, %1, 1\n\t"
        "s_cbranch_scc0 Lsp%=\n"
        "Lse%=:"
        : "=&v"(vt), "=&s"(cn) : "v"(vaddr), "s"(need) : "memory", "scc", "vcc");
}
DEV void g_poll1(rsrc_t rs, u32 voff, u32 tag) {
    u32 vt, cn;
    asm volatile(
        "s_mov_b32 %1, 0x8000\n"
        "Lgp%=:\n\t"
        "buffer_load_dword %0, %2, %3, 0 offen sc1\n\t"
        "s_waitcnt vmcnt(0)\n\t"
        "v_cmp_ne_u32 vcc, %4, %0\n\t"
        "s_cbranch_vccz Lge%=\n\t"
        "s_sleep 2\n\t"
        "s_sub_u32 %1, %1, 1\n\t"
        "s_cbranch_scc0 Lgp%=\n"
        "Lge%=:"
        : "=&v"(vt), "=&s"(cn) : "v"(voff), "s"(rs), "s"(tag) : "memory", "scc", "vcc");
}

struct Args { const u16 *x, *k, *v, *sk, *hk, *dv, *wg, *wu, *wd; u16* out; char* ws; };

DEV void kv_tile(const Args& A, rsrc_t rw, int tile, u32 lb, int lane) {
    const int rtile = tile / 10, cg = tile - rtile * 10, r0 = rtile * 64;
    const int row = lane >> 3;
    rsrc_t rk = mkrs(A.k, S * D * 2), rv = mkrs(A.v, S * D * 2), rs = mkrs(A.sk, D * 8);
    const u32 vo = (r0 + row) * (D * 2) + cg * 128 + (lane & 7) * 16;
    dma16(lb, (row >= 5 && r0 > 0) ? vo - 8 * D * 2 : OOB, rk, 0);
#pragma unroll
    for (int i = 0; i < 8; ++i) dma16(lb + 1024 + i * 1024, vo, rk, i * 8 * D * 2);
#pragma unroll
    for (int i = 0; i < 8; ++i) dma16(lb + 9216 + i * 1024, vo, rv, i * 8 * D * 2);
    u32x2 w[4];
#pragma unroll
    for (int cb = 0; cb < 4; ++cb) w[cb] = ld64(rs, (cg * 64 + (lane & 15)) * 8, cb * 128);
    WAITV(0);
    const int g = lane >> 4, q = (lane >> 2) & 3, p = lane & 3;
    const u32 ta = lb + (8 * g + q) * 128 + p * 8;
    const u32 so = WS_KV + (cg * 64 + (lane & 15)) * 4096 + (2040 - r0 - 8 * g) * 2;
#pragma unroll
    for (int cb = 0; cb < 4; ++cb) {
        const float w0 = blo(w[cb][0]), w1 = bhi(w[cb][0]), w2 = blo(w[cb][1]), w3 = bhi(w[cb][1]);
#pragma unroll
        for (int hf = 0; hf < 2; ++hf) {
            const u32 a = ta + cb * 32 + hf * 4096;
            u32x2 k0 = ldstru(a + 512), k1 = ldstru(a + 1024), k2 = ldstru(a + 1536), v0 = ldstru(a + 9216), v1 = ldstru(a + 9216 + 512);
            float kk[11] = {bhi(k0[0]), blo(k0[1]), bhi(k0[1]), blo(k1[0]), bhi(k1[0]), blo(k1[1]), bhi(k1[1]), blo(k2[0]), bhi(k2[0]), blo(k2[1]), bhi(k2[1])};
            float vv[8] = {blo(v0[0]), bhi(v0[0]), blo(v0[1]), bhi(v0[1]), blo(v1[0]), bhi(v1[0]), blo(v1[1]), bhi(v1[1])};
            float o[8];
#pragma unroll
            for (int i = 0; i < 8; ++i) o[i] = __builtin_fmaf(kk[i + 3], w0, __builtin_fmaf(kk[i + 2], w1, __builtin_fmaf(kk[i + 1], w2, kk[i] * w3))) * vv[i];
            u32x4 ov = {pk2(o[7], o[6]), pk2(o[5], o[4]), pk2(o[3], o[2]), pk2(o[1], o[0])};
            st128(ov, so - hf * 64, rw, cb * 16 * 4096);
        }
    }
}
DEV void wd_issue(const Args& A, int u, u32 lb, int lane) {
    const int s = u / 10, lg = u - s * 10;
    rsrc_t rd = mkrs(A.wd, DF * D * 2);
    const u32 vo = (32 * s + (lane >> 3)) * (D * 2) + lg * 128 + (lane & 7) * 16;
#pragma unroll
    for (int i = 0; i < 4; ++i) dma16(lb + i * 1024, vo, rd, i * 8 * D * 2);
}
DEV void wd_finish(rsrc_t rw, int u, u32 lb, int lane) {
    const int s = u / 10, lg = u - s * 10;
    const u32 ta = lb + (lane >> 2) * 128 + (lane & 3) * 8;
#pragma unroll
    for (int f = 0; f < 4; ++f) {
        u32x2 a = ldstru(ta + f * 32), b = ldstru(ta + f * 32 + 2048);
        st128(u32x4{a[0], a[1], b[0], b[1]}, lane * 16, rw, WS_WD + ((4 * lg + f) * 54 + s) * 1024);
    }
}
template <int CTRL> DEV float dppadd(float v) { return v + __uint_as_float(__builtin_amdgcn_update_dpp(0u, __float_as_uint(v), CTRL, 0xf, 0xf, true)); }
DEV void row_stats(const Args& A, rsrc_t rw, int b, int lane) {
    rsrc_t rx = mkrs(A.x, S * D * 2);
    u32x4 v[10];
#pragma unroll
    for (int i = 0; i < 10; ++i) v[i] = ld128(rx, (b * 8 + (lane >> 3)) * (D * 2) + (lane & 7) * 16, i * 128);
    float s1 = 0.f, s2 = 0.f;
#pragma unroll
    for (int i = 0; i < 10; ++i)
#pragma unroll
        for (int j = 0; j < 4; ++j) { s1 = dot2(v[i][j], 0x3F803F80u, s1); s2 = dot2(v[i][j], v[i][j], s2); }
    s1 = dppadd<0xB1>(s1); s2 = dppadd<0xB1>(s2);
    s1 = dppadd<0x4E>(s1); s2 = dppadd<0x4E>(s2);
    s1 = dppadd<0x141>(s1); s2 = dppadd<0x141>(s2);
    float mean = s1 * (1.f / D);
    float var = fmaxf(__builtin_fmaf(-mean, mean, s2 * (1.f / D)), 0.f);
    float den = bfr(__builtin_sqrtf(bfr(bfr(var) + 1e-5f)));
    st64(u32x2{__float_as_uint(bfr(mean)), __float_as_uint(den)}, (lane & 7) ? OOB : WS_ST + (b * 8 + (lane >> 3)) * 8, rw, 0);
}
DEV void col_sums(const Args& A, rsrc_t rw, int grp, int rt, int lane) {
    rsrc_t rg = mkrs(A.wg, D * DF * 2), ru = mkrs(A.wu, D * DF * 2);
    u32x4 g[9], u[9];
    const u32 vo = (rt * 72 + (lane >> 3)) * (DF * 2) + grp * 128 + (lane & 7) * 16;
#pragma unroll
    for (int i = 0; i < 9; ++i) { g[i] = ld128(rg, vo + i * 8 * DF * 2, 0); u[i] = ld128(ru, vo + i * 8 * DF * 2, 0); }
    float cg[8], cu[8];
    u32 klo = 0x00003F80u, khi = 0x3F800000u;
    asm volatile("" : "+s"(klo), "+s"(khi));
#pragma unroll
    for (int j = 0; j < 8; ++j) cg[j] = cu[j] = 0.f;
#pragma unroll
    for (int i = 0; i < 9; ++i)
#pragma unroll
        for (int j = 0; j < 4; ++j) {
            cg[2 * j] = dot2(g[i][j], klo, cg[2 * j]); cg[2 * j + 1] = dot2(g[i][j], khi, cg[2 * j + 1]);
            cu[2 * j] = dot2(u[i][j], klo, cu[2 * j]); cu[2 * j + 1] = dot2(u[i][j], khi, cu[2 * j + 1]);
        }
    float t[8];
#pragma unroll
    for (int j = 0; j < 8; ++j) { auto r = __builtin_amdgcn_permlane32_swap(__float_as_uint(cg[j]), __float_as_uint(cu[j]), false, false); t[j] = __uint_as_float(r[0]) + __uint_as_float(r[1]); }
    u32x4 o;
#pragma unroll
    for (int j = 0; j < 4; ++j) { auto r = __builtin_amdgcn_permlane16_swap(__float_as_uint(t[j]), __float_as_uint(t[j + 4]), false, false);
        o[j] = __float_as_uint(dppadd<0x128>(__uint_as_float(r[0]) + __uint_as_float(r[1]))); }
    st128(o, (lane & 8) ? OOB : WS_CS + (rt * 3456 + grp * 128 + (lane >> 5) * 64 + (lane & 7) * 8 + ((lane >> 4) & 1) * 4) * 4, rw, 0);
}
DEV void epi_prep(rsrc_t rw, int grp, int blk0, int lane) {
    u32x4 sv[2]; u32x2 cv[9];
#pragma unroll
    for (int i = 0; i < 2; ++i) sv[i] = ld128c(rw, WS_ST + blk0 * 128 + lane * 16, i * 1024);
#pragma unroll
    for (int i = 0; i < 9; ++i) cv[i] = ld64c(rw, WS_CS + grp * 512 + lane * 8, i * 3456 * 4);
    const float K1 = -2.f * 1.4426950408889634f * 0.7978845608028654f;
#pragma unroll
    for (int i = 0; i < 2; ++i)
#pragma unroll
        for (int j = 0; j < 2; ++j) {
            float m = __uint_as_float(sv[i][2 * j]), den = __uint_as_float(sv[i][2 * j + 1]);
            float r = __builtin_amdgcn_rcpf(den);
            if (i == 0 || lane < 56) sts128f(EPI + (i * 128 + lane * 2 + j) * 16, f32x4{-m, K1 * r, K1 * 0.044715f * r * r * r, den * den});
        }
    float c0 = 0.f, c1 = 0.f;
#pragma unroll
    for (int i = 0; i < 9; ++i) { c0 += __uint_as_float(cv[i][0]); c1 += __uint_as_float(cv[i][1]); }
    *(AS3 f32x2*)(uintptr_t)(ECS + lane * 8) = f32x2{c0, c1};
}

DEV void g1_compute(int c, int lane, rsrc_t rw, u32 hbase, int nblk) {
    const int wi = c >> 1, wj = c & 1;
    const int tok = lane & 15, kq = lane >> 4;
    u32 xa[2], xe[2], wa[2];
    {
        int q = (lane >> 2) & 3, p = lane & 3;
        int f = ((q >> 1) & 1) | ((kq & 1) << 1);
#pragma unroll
        for (int e = 0; e < 2; ++e) {
            wa[e] = XB + (8 * kq + q) * 128 + (((2 * wj + (wi ^ e)) ^ f) << 5) + p * 8;
            u32 t = tok * 128 + (((4 * e + kq) ^ (tok >> 1)) << 4);
            xa[e] = t + wi * 7 * 2048; xe[e] = t + 14 * 2048;
        }
    }
    f32x4 acc[8][4];
#pragma unroll
    for (int b = 0; b < 8; ++b)
#pragma unroll
        for (int u = 0; u < 4; ++u) acc[b][u] = f32x4{0.f, 0.f, 0.f, 0.f};
    bf16x8 xf[2][8], wf[2][4];
    auto ldf = [&](int ks) {
#pragma unroll
        for (int u = 0; u < 4; ++u) { u32 a = wa[u & 1] + ks * 4096 + (u >> 1) * 8192; wf[ks][u] = cat(ldstr(a), ldstr(a + 512)); }
#pragma unroll
        for (int b = 0; b < 7; ++b) xf[ks][b] = lds128(xa[ks] + b * 2048);
        xf[ks][7] = lds128(xe[ks]);
    };
    auto mm = [&](int ks) {
#pragma unroll
        for (int b = 0; b < 7; ++b)
#pragma unroll
            for (int u = 0; u < 4; ++u) acc[b][u] = MFMA16(wf[ks][u], xf[ks][b], acc[b][u]);
        acc[7][0] = MFMA16(wf[ks][0], xf[ks][7], acc[7][0]);
        acc[7][2] = MFMA16(wf[ks][2], xf[ks][7], acc[7][2]);
    };
    auto pin = [&]() {
#pragma unroll
        for (int b = 0; b < 7; ++b) asm volatile("" : "+v"(acc[b][0]), "+v"(acc[b][1]), "+v"(acc[b][2]), "+v"(acc[b][3]));
        asm volatile("" : "+v"(acc[7][0]), "+v"(acc[7][2]));
        __builtin_amdgcn_sched_barrier(0);
    };
    lds_spin(L_CNT0, 6);
    u32 fa = L_FULL + 4, fw = L_FREE + c * 4, cnt = 1;
    asm volatile("" : "+v"(fa), "+v"(fw), "+v"(cnt));
    int slot = 0;
    u32 have, need = 2, fprev;
    auto halfA = [&]() {
        ldf(1);
        ldsflagw(fw, cnt);
        u32 f = ldsflag1(fa);
        int d = slot == 2 ? -2 * (int)SLOT : (int)SLOT;
        slot = slot == 2 ? 0 : slot + 1;
        mm(0);
        have = rfl(f);
#pragma unroll
        for (int e = 0; e < 2; ++e) { xa[e] += d; xe[e] += d; wa[e] += d; }
        fprev = fa; fa ^= 4; cnt += 1;
#pragma unroll
        for (int i = 0; i < 16; ++i) { __builtin_amdgcn_sched_group_barrier(0x008, 1, 0); __builtin_amdgcn_sched_group_barrier(0x100, 1, 0); }
        __builtin_amdgcn_sched_group_barrier(0x008, 1, 0); __builtin_amdgcn_sched_group_barrier(0x200, 1, 0);
        __builtin_amdgcn_sched_group_barrier(0x008, 1, 0); __builtin_amdgcn_sched_group_barrier(0x100, 1, 0);
#pragma unroll
        for (int i = 0; i < 8; ++i) { __builtin_amdgcn_sched_group_barrier(0x008, 1, 0); __builtin_amdgcn_sched_group_barrier(0x002, 1, 0); }
        __builtin_amdgcn_sched_group_barrier(0x008, 1, 0);
        __builtin_amdgcn_sched_group_barrier(0x002, 1, 0);
        __builtin_amdgcn_sched_group_barrier(0x008, 3, 0);
        asm volatile("" : "+v"(xa[0]), "+v"(xa[1]), "+v"(xe[0]), "+v"(xe[1]), "+v"(wa[0]), "+v"(wa[1]), "+v"(fa), "+v"(cnt));
        pin();
    };
    auto halfB = [&]() {
        ldf(0);
        mm(1);
#pragma unroll
        for (int i = 0; i < 16; ++i) { __builtin_amdgcn_sched_group_barrier(0x008, 1, 1); __builtin_amdgcn_sched_group_barrier(0x100, 1, 1); }
        __builtin_amdgcn_sched_group_barrier(0x008, 14, 1);
        pin();
    };
    ldf(0);
#pragma unroll 1
    for (int st = 0; st < 10; ++st) {
        halfA();
        lds_wait_ge(have, need, fprev);
        need = need == 10u ? 0u : need + 1;
        halfB();
    }
    lds_spin(L_EPI, 1);
    f32x4 cs[4];
#pragma unroll
    for (int u = 0; u < 4; ++u) cs[u] = lds128f(ECS + (u >> 1) * 256 + (2 * wj + (wi ^ (u & 1))) * 64 + kq * 16);
    auto act2 = [&](const f32x4& e, const f32x4& g, const f32x4& u, const f32x4& cg, const f32x4& cu, int i) {
        float r[2];
#pragma unroll
        for (int j = 0; j < 2; ++j) {
            float a = __builtin_fmaf(e[0], cg[i + j], g[i + j]), b = __builtin_fmaf(e[0], cu[i + j], u[i + j]);
            float p = __builtin_fmaf(a * a, e[2], e[1]);
            float ex = __builtin_amdgcn_exp2f(a * p);
            r[j] = (a * b) * __builtin_amdgcn_rcpf(__builtin_fmaf(ex, e[3], e[3]));
        }
        return pk2(r[0], r[1]);
    };
#pragma unroll
    for (int b = 0; b < 7; ++b) {
        f32x4 e = lds128f(EPI + ((wi * 7 + b) * 16 + tok) * 16);
        u32 o[4];
#pragma unroll
        for (int h = 0; h < 2; ++h) { o[2 * h] = act2(e, acc[b][h], acc[b][2 + h], cs[h], cs[2 + h], 0); o[2 * h + 1] = act2(e, acc[b][h], acc[b][2 + h], cs[h], cs[2 + h], 2); }
        u32x4 ov = wi ? u32x4{o[2], o[3], o[0], o[1]} : u32x4{o[0], o[1], o[2], o[3]};
        st128(ov, lane * 16, rw, hbase + ((wi * 7 + b) * 54 + wj) * 1024);
    }
    if (nblk == 15) {
        f32x4 e = lds128f(EPI + (14 * 16 + tok) * 16);
        u32x2 ov = {act2(e, acc[7][0], acc[7][2], cs[0], cs[2], 0), act2(e, acc[7][0], acc[7][2], cs[0], cs[2], 2)};
        st64(ov, lane * 16, rw, hbase + (14 * 54 + wj) * 1024 + wi * 8);
    }
}
struct G1Src { rsrc_t rx, rg, ru; u32 xv0, xv1, wv0, wv1; };
DEV void g1_loader(int e, const G1Src& g, int lane) {
    int slot = e ? 1 : 2;
    const u32 fr = L_FREE + (lane & 3) * 4;
#pragma unroll 1
    for (int st = e ? 1 : 2; st < 10; st += 2) {
        if (st > 2) lds_spin(fr, st - 2);
        const u32 so = slot * SLOT;
#pragma unroll
        for (int p = 0; p < 46; ++p) {
            if (p < 16) dma16(so + XB + p * 1024, (p & 1) ? g.wv1 : g.wv0, (p >> 3) ? g.ru : g.rg, st * (64 * DF * 2) + (p & 7) * 8 * DF * 2);
            else dma16(so + (p - 16) * 1024, (p & 1) ? g.xv1 : g.xv0, g.rx, st * 128 + (p - 16) * 8 * D * 2);
        }
        WAITV(0);
        ldsflagw(L_FULL + e * 4, st + 1);
        slot = slot == 0 ? 2 : slot - 1;
    }
}

DEV void conv_image(const Args& A, rsrc_t rw, int ch, u32 ib, int lane) {
    rsrc_t rh = mkrs(A.hk, D * S * 2);
    u32x4 v[4];
#pragma unroll
    for (int i = 0; i < 4; ++i) v[i] = ld128c(rw, WS_KV + lane * 16, ch * 4096 + i * 1024);
    lds_spin(L_FREE + (lane & 3) * 4, 8);
#pragma unroll
    for (int i = 0; i < 4; ++i) dma16(ib + 64 + i * 1024, lane * 16, rh, ch * 4096 + i * 1024);
    const u32 ua = ib + (lane >> 4) * 320 + (lane & 15) * 16;
#pragma unroll
    for (int i = 0; i < 4; ++i) {
        u32 sel = lane == 0 ? (i < 3 ? v[i < 3 ? i + 1 : 0][0] : 0u) : v[i][0];
        u32 nx = __builtin_amdgcn_ds_bpermute(((lane + 1) & 63) * 4, sel);
        sts128(ua + IMG_U0 + i * 1280, v[i]);
        sts128(ua + IMG_U1 + i * 1280, u32x4{__builtin_amdgcn_alignbit(v[i][1], v[i][0], 16), __builtin_amdgcn_alignbit(v[i][2], v[i][1], 16),
                                             __builtin_amdgcn_alignbit(v[i][3], v[i][2], 16), __builtin_amdgcn_alignbit(nx, v[i][3], 16)});
    }
    if (lane < 36) sts128(lane < 32 ? ib + (lane < 16 ? IMG_U0 : IMG_U1) + 5120 + (lane & 15) * 16 : ib + (lane - 32) * 16, u32x4{0u, 0u, 0u, 0u});
    WAITV(0);
}
DEV bf16x8 ldsF(u32 a) { return asbf(u32x4{lds32(a), lds32(a + 4), lds32(a + 8), lds32(a + 12)}); }
struct WdDma { int i, ok, base, n, rot; u32 lds, src; };
DEV void wd_dma_step(WdDma& w, rsrc_t rw, int lane) {
    int st = w.i + w.rot; st = st >= w.n ? st - w.n : st;
#pragma unroll
    for (int f = 0; f < 5; ++f) dma16c(w.lds + (w.i * 5 + f) * 1024, lane * 16, rw, w.src + (f * 54 + w.base + st) * 1024);
    ++w.i;
}
DEV void conv_task(const Args& A, rsrc_t rw, int ch, int g, u32 ib, u32 yst, int lane, WdDma& wd) {
    const int a = lane & 15, q = lane >> 4;
    u32 ha = ib + 4 + 4 * a + 16 * q;
    const u32 ub = ib + ((a & 1) ? IMG_U0 : IMG_U1) + 16 * q;
    u32 ua = ub + (15 - 8 * g - (a >> 1)) * 320;
    const u32 uz = ub + 5120;
    const float dv = blo(A.dv[ch]);
    f32x4 acc[4];
    bf16x8 f[4], u[4];
#pragma unroll
    for (int w = 0; w < 4; ++w) { acc[w] = f32x4{0.f, 0.f, 0.f, 0.f}; f[w] = asbf(u32x4{0u, 0u, 0u, 0u}); }
    f[0] = ldsF(ha);
    u[0] = lds128(ua); u[1] = lds128(ua + 64);
    const int nit = 8 * (g + 1);
#pragma unroll 1
    for (int it = 0; it < nit; ++it) {
        const u32 un = min(ua + 320, uz);
#pragma unroll
        for (int t = 0; t < 4; ++t) {
            acc[0] = MFMA16(u[t], f[(t + 1) & 3], acc[0]);
            u[(t + 2) & 3] = lds128(t < 2 ? ua + (t + 2) * 64 : un + (t - 2) * 64);
            f[(t + 1) & 3] = ldsF(ha + (t + 1) * 64);
            acc[1] = MFMA16(u[t], f[(t + 2) & 3], acc[1]);
            acc[2] = MFMA16(u[t], f[(t + 3) & 3], acc[2]);
            acc[3] = MFMA16(u[t], f[t], acc[3]);
            __builtin_amdgcn_sched_barrier(0);
        }
        ua = un; ha += 256;
        if (wd.i < 4) {
            if (!wd.ok) wd.ok = __builtin_amdgcn_ballot_w64(ldsflag1(L_FREE + (lane & 3) * 4) < 10u) == 0;
            if (wd.ok) wd_dma_step(wd, rw, lane);
        }
    }
#pragma unroll
    for (int w = 0; w < 4; ++w)
#pragma unroll
        for (int ih = 0; ih < 2; ++ih) {
            const int sl = 32 * w + 2 * a + 128 * (2 * q + ih);
            const int e = 2046 - 1024 * g - sl;
            const u32 uu = lds32(ib + IMG_U0 + (e >> 7) * 320 + (e & 127) * 2);
            sts32(yst + sl * 2, pk2(bfr(acc[w][2 * ih]) + bfr(bhi(uu) * dv), bfr(acc[w][2 * ih + 1]) + bfr(blo(uu) * dv)));
        }
    asm volatile("" ::: "memory");
#pragma unroll
    for (int i = 0; i < 2; ++i) st128(lds128u(yst + i * 1024 + lane * 16), lane * 16, rw, WS_Y + ch * 4096 + g * 2048 + i * 1024);
}

DEV u32 a_area(int w) { return w < 2 ? 102400 + w * 20480 : (w - 2) * 20480; }
DEV int wrapn(int v, int n) { return v >= n ? v - n : v; }
template <int NS> DEV void g2_wload(bf16x8 (&wr)[NS][5], rsrc_t rw, u32 src, int i0, int base, int n, int rot, int lane) {
#pragma unroll
    for (int i = 0; i < NS; ++i)
#pragma unroll
        for (int f = 0; f < 5; ++f) wr[i][f] = asbf(ld128c(rw, lane * 16, src + (f * 54 + base + wrapn(i0 + i + rot, n)) * 1024));
}
DEV void g2_b(const Args& A, rsrc_t rw, const bf16x8 (&wr)[6][5], int w, int rot, int blk, int cgp, u32 tag, int lane) {
    f32x4 acc[4][5];
#pragma unroll
    for (int t = 0; t < 4; ++t)
#pragma unroll
        for (int f = 0; f < 5; ++f) acc[t][f] = f32x4{0.f, 0.f, 0.f, 0.f};
    bf16x8 hf[6][4];
    auto ldh = [&](int i) {
#pragma unroll
        for (int t = 0; t < 4; ++t) hf[i][t] = asbf(ld128c(rw, lane * 16, WS_H + ((blk + ((t + w) & 3)) * 54 + 6 * w + wrapn(i + rot, 6)) * 1024));
    };
    ldh(0); ldh(1);
#pragma unroll
    for (int i = 0; i < 6; ++i) {
#pragma unroll
        for (int t = 0; t < 4; ++t)
#pragma unroll
            for (int f = 0; f < 5; ++f) acc[t][f] = MFMA16(wr[i][f], hf[i][t], acc[t][f]);
        __builtin_amdgcn_sched_barrier(0);
        if (i + 2 < 6) ldh(i + 2);
        __builtin_amdgcn_sched_barrier(0);
    }
    const int tok = lane & 15, q = lane >> 4, row = (blk + w) * 16 + tok;
    rsrc_t rx = mkrs(A.x, S * D * 2);
    u32x2 xv[5];
#pragma unroll
    for (int f = 0; f < 5; ++f) xv[f] = ld64(rx, row * (D * 2) + cgp * 160 + q * 8, f * 32);
    const u32x2 sv = ld64c(rw, WS_ST + row * 8, 0);
    lds_spin(L_YCNT, 4);
    if (w == 3) {
        g_poll1(rw, WS_F2 + cgp * 128 + (lane & 31) * 4, tag);
#pragma unroll
        for (int i = 0; i < 10; ++i) dma16c(YT + i * 1024, (lane >> 3) * 4096 + (lane & 7) * 16, rw, WS_Y + (cgp * 80 + i * 8) * 4096 + blk * 32);
    }
#pragma unroll
    for (int t = 1; t < 4; ++t)
#pragma unroll
        for (int f = 0; f < 5; ++f) sts128f(RB + ((((t + w) & 3) * 3 + t - 1) * 5 + f) * 1024 + lane * 16, acc[t][f]);
    lds_add(L_BD);
    if (w == 3) { WAITV(0); ldsflagw(L_YRDY, 1u); }
    lds_spin(L_BD, 4);
#pragma unroll
    for (int j = 0; j < 3; ++j)
#pragma unroll
        for (int f = 0; f < 5; ++f) acc[0][f] += lds128f(RB + ((w * 3 + j) * 5 + f) * 1024 + lane * 16);
    lds_spin(L_YRDY, 1);
    const float m = __uint_as_float(sv[0]), r = __builtin_amdgcn_rcpf(__uint_as_float(sv[1]));
#pragma unroll
    for (int f = 0; f < 5; ++f) {
        const u32x2 y = ldstru(YT + (16 * f + (lane >> 2)) * 128 + w * 32 + (lane & 3) * 8);
        const float xx[4] = {blo(xv[f][0]), bhi(xv[f][0]), blo(xv[f][1]), bhi(xv[f][1])}, yy[4] = {blo(y[0]), bhi(y[0]), blo(y[1]), bhi(y[1])};
#pragma unroll
        for (int i = 0; i < 4; ++i) acc[0][f][i] += bfr(xx[i] + bfr(bfr(bfr(xx[i] - m) * r) * yy[i]));
    }
    lds_spin(L_AD, 4);
    rsrc_t ro = mkrs(A.out, S * D * 2);
#pragma unroll
    for (int f = 0; f < 5; ++f) {
#pragma unroll
        for (int j = 0; j < 4; ++j) acc[0][f] += lds128f(a_area(j) + (w * 5 + f) * 1024 + lane * 16);
        __builtin_amdgcn_raw_buffer_store_b64(u32x2{pk2(acc[0][f][0], acc[0][f][1]), pk2(acc[0][f][2], acc[0][f][3])}, ro, row * (D * 2) + cgp * 160 + q * 8 + f * 32, 0, 0);
    }
}
DEV void g2_a(rsrc_t rw, const bf16x8 (&wr)[4][5], int w, int base, int n, int rot, int blk, int lane) {
    f32x4 acc[4][5];
#pragma unroll
    for (int t = 0; t < 4; ++t)
#pragma unroll
        for (int f = 0; f < 5; ++f) acc[t][f] = f32x4{0.f, 0.f, 0.f, 0.f};
    bf16x8 hf[8][4], wl[4][5];
    const u32 la = a_area(w) + lane * 16;
    auto ldh = [&](int i) {
        const u32 vo = (i == 7 && n == 7) ? OOB : (u32)lane * 16;
#pragma unroll
        for (int t = 0; t < 4; ++t) hf[i][t] = asbf(ld128c(rw, vo, WS_H + ((blk + t) * 54 + base + wrapn(i + rot, n)) * 1024));
    };
    auto ldw = [&](int i) {
#pragma unroll
        for (int f = 0; f < 5; ++f) wl[i][f] = lds128(la + (i * 5 + f) * 1024);
    };
    ldh(0); ldh(1);
    lds_spin(L_WDL + w * 4, 1);
    ldw(0);
#pragma unroll
    for (int i = 0; i < 8; ++i) {
        if (i + 1 < 4) ldw(i + 1);
        __builtin_amdgcn_sched_barrier(0);
#pragma unroll
        for (int t = 0; t < 4; ++t)
#pragma unroll
            for (int f = 0; f < 5; ++f) acc[t][f] = MFMA16(i < 4 ? wl[i & 3][f] : wr[i & 3][f], hf[i][t], acc[t][f]);
        __builtin_amdgcn_sched_barrier(0);
        if (i + 2 < 8) ldh(i + 2);
        __builtin_amdgcn_sched_barrier(0);
    }
#pragma unroll
    for (int t = 0; t < 4; ++t)
#pragma unroll
        for (int f = 0; f < 5; ++f) sts128f(la + (t * 5 + f) * 1024, acc[t][f]);
    lds_add(L_AD);
}

__global__ __launch_bounds__(512, 2) void fused_kernel(Args A) {
    const int b = blockIdx.x, lane = threadIdx.x & 63, wid = rfl(threadIdx.x >> 6);
    const int xcd = b & 7, idx = b >> 3;
    rsrc_t rw = mkrs(A.ws, WS_SIZE);
    int grp = 0, rt = 0, idle = -1;
    if (xcd < 6) {
        int c = (xcd & 1) * 32 + idx;
        if (c == 63) idle = xcd >> 1;
        grp = c / 3; rt = (xcd >> 1) * 3 + (c - grp * 3);
    } else if (xcd == 6) {
        if (idx >= 30) idle = idx - 27;
        int gi = idx / 5; grp = 21 + gi; rt = idx - gi * 5;
    } else {
        if (idx >= 24) idle = idx - 19;
        grp = 21 + (idx >> 2); rt = 5 + (idx & 3);
    }
    const bool busy = idle < 0;
    if (threadIdx.x < 32) ldsflagw(FLG + lane * 4, (!busy && lane >= 4 && lane < 8) ? 10u : 0u);
    const int blk0 = rt * 14 + (rt > 7 ? 1 : 0), nblk = rt >= 7 ? 15 : 14;
    G1Src g;
    g.rx = mkrs(A.x, S * D * 2); g.rg = mkrs(A.wg, D * DF * 2); g.ru = mkrs(A.wu, D * DF * 2);
    { int row = lane >> 3;
      g.xv0 = (blk0 * 16 + row) * (D * 2) + (((lane & 7) ^ (row >> 1)) << 4); g.xv1 = g.xv0 ^ 64;
      g.wv0 = row * (DF * 2) + grp * 128 + (((lane & 7) ^ (((row >> 1) & 1) << 1)) << 4); g.wv1 = g.wv0 ^ 64; }
    if (busy && wid != 4 && wid != 5) {
        const int w6 = wid < 4 ? wid : wid - 2;
        if (w6 < 2) {
            rsrc_t rs = w6 ? g.ru : g.rg;
#pragma unroll
            for (int i = 0; i < 8; ++i) dma16(XB + w6 * 8192 + i * 1024, (i & 1) ? g.wv1 : g.wv0, rs, i * 8 * DF * 2);
        } else {
#pragma unroll
            for (int i = 0; i < 8; ++i) if (i < 6 || w6 < 5) dma16(((w6 - 2) * 8 + i) * 1024, (i & 1) ? g.xv1 : g.xv0, g.rx, ((w6 - 2) * 8 + i) * 8 * D * 2);
        }
    }
    BARL();
    if (wid == 7) ldsflagw(L_TAG, __builtin_amdgcn_raw_buffer_load_b32(rw, 0, 0, 16) + 0x9E3779B1u);
    if (busy && wid != 4 && wid != 5) { WAITV(0); lds_add(L_CNT0); }
    if (idle >= 0) {
        const u32 lb = wid * 17408;
        const int tile = 243 + idle * 6 + wid;
        if (wid < 6 && tile < 320) kv_tile(A, rw, tile, lb, lane);
        if (wid == 7) row_stats(A, rw, b, lane);
        const int u0 = 243 + (idle * 8 + wid) * 3;
        WAITL();
#pragma unroll
        for (int i = 0; i < 3; ++i) wd_issue(A, min(u0 + i, 539), lb + i * 4096, lane);
        WAITV(0);
#pragma unroll
        for (int i = 0; i < 3; ++i) wd_finish(rw, min(u0 + i, 539), lb + i * 4096, lane);
        WAITV(0);
        if (lds_add(L_P0CNT) == 7u && lane == 0) st32(ldsflag1(L_TAG), WS_F0 + b * 4, rw);
    } else if (wid >= 6) {
        const int q = rt * 27 + grp;
        if (wid == 6) kv_tile(A, rw, q, STG, lane);
        else {
            wd_issue(A, q, STG + 17408, lane);
            row_stats(A, rw, b, lane);
            col_sums(A, rw, grp, rt, lane);
            WAITV(0);
            wd_finish(rw, q, STG + 17408, lane);
        }
        WAITV(0);
        if (lds_add(L_P0CNT) == 1u && lane == 0) st32(ldsflag1(L_TAG), WS_F0 + b * 4, rw);
    }
    const int cgp = idx & 7, blk = (((0x52764310u >> (xcd * 4)) & 7) * 4 + (idx >> 3)) * 4;
    const u32 wsrc = WS_WD + cgp * 5 * 54 * 1024;
    const int w4 = wid & 3, an = w4 < 2 ? 8 : 7, abase = 24 + w4 * 8 - (w4 == 3 ? 1 : 0), arot = wrapn(cgp, an);
    if (wid < 4) {
        if (busy) {
            g1_compute(wid, lane, rw, WS_H + (blk0 * 54 + grp * 2) * 1024, nblk);
            WAITV(0);
            if (lds_add(L_HCNT) == 3u && lane == 0) st32(ldsflag1(L_TAG), WS_F1 + (rt * 32 + grp) * 4, rw);
        } else lds_spin(L_P0ALL, 1);
        bf16x8 wr[4][5];
        g2_wload<4>(wr, rw, wsrc, 4, abase, an, arot, lane);
        if (wid == 0) {
            const int bk = blk + (lane < 32 ? 0 : 3), r = bk < 98 ? bk / 14 : (bk < 113 ? 7 : 8);
            g_poll1(rw, WS_F1 + (r * 32 + min(lane & 31, 26)) * 4, rfl(ldsflag1(L_TAG)));
            ldsflagw(L_HRDY, 1u);
        } else lds_spin(L_HRDY, 1);
        g2_a(rw, wr, wid, abase, an, arot, blk, lane);
    } else {
        if (wid < 6) { if (busy) g1_loader(5 - wid, g, lane); }
        if (wid == 7) {
            const u32 tg = rfl(ldsflag1(L_TAG));
#pragma unroll
            for (int i = 0; i < 4; ++i) g_poll1(rw, WS_F0 + i * 256 + lane * 4, tg);
            ldsflagw(L_P0ALL, 1u);
            if (b == 0 && lane == 0) st32(tg, 0, rw);
            if (busy) { epi_prep(rw, grp, blk0, lane); WAITL(); ldsflagw(L_EPI, 1u); }
        } else lds_spin(L_P0ALL, 1);
        const u32 tag = rfl(ldsflag1(L_TAG));
        const int brot = wrapn(cgp, 6);
        bf16x8 wr[6][5];
        WdDma wd = {0, 0, abase, an, arot, a_area(w4), wsrc};
        const int pr = (b >> 4) * 8 + xcd, mem = idx & 1;
        const u32 yst = YST + w4 * 2048;
        if (wid < 7) {
            const int ch = pr * 5 + (mem ? (w4 == 2 ? 2 : w4 + 3) : w4);
            const u32 ib = SLOT + w4 * IMGSZ;
            conv_image(A, rw, ch, ib, lane);
            WAITL();
            ldsflagw(L_IMG + w4 * 4, 1u);
            g2_wload<6>(wr, rw, wsrc, 0, 6 * w4, 6, brot, lane);
            conv_task(A, rw, ch, (mem && w4 == 2) ? 0 : 1, ib, yst, lane, wd);
        } else {
            g2_wload<6>(wr, rw, wsrc, 0, 6 * w4, 6, brot, lane);
#pragma unroll 1
            for (int sl = 0; sl < 2; ++sl) {
                lds_spin(L_IMG + sl * 4, 1);
                conv_task(A, rw, pr * 5 + mem * 3 + sl, 0, SLOT + sl * IMGSZ, yst, lane, wd);
            }
        }
        if (wd.i < 4) {
            lds_spin(L_FREE + (lane & 3) * 4, 10);
#pragma unroll 1
            while (wd.i < 4) wd_dma_step(wd, rw, lane);
        }
        WAITV(0);
        ldsflagw(L_WDL + w4 * 4, 1u);
        if (lds_add(L_YCNT) == 3u && lane == 0) st32(tag, WS_F2 + (pr * 2 + mem) * 4, rw);
        lds_spin(L_HRDY, 1);
        g2_b(A, rw, wr, w4, brot, blk, cgp, tag, lane);
    }
}

extern "C" void kernel_launch(void* const* d_in, const int* in_sizes, int n_in, void* d_out, int out_size, void* d_ws, size_t ws_size, hipStream_t stream) {
    Args A;
    A.x = (const u16*)d_in[0]; A.k = (const u16*)d_in[1]; A.v = (const u16*)d_in[2]; A.sk = (const u16*)d_in[3]; A.hk = (const u16*)d_in[4];
    A.dv = (const u16*)d_in[5]; A.wg = (const u16*)d_in[7]; A.wu = (const u16*)d_in[8]; A.wd = (const u16*)d_in[9];
    A.out = (u16*)d_out; A.ws = (char*)d_ws;
    (void)hipFuncSetAttribute((const void*)fused_kernel, hipFuncAttributeMaxDynamicSharedMemorySize, LDS_BYTES);
    fused_kernel<<<256, 512, LDS_BYTES, stream>>>(A);
}
```

```cpp
#include <hip/hip_runtime.h>
#include <stdint.h>
typedef unsigned short u16;
typedef unsigned int u32;
typedef __attribute__((ext_vector_type(8))) __bf16 bf16x8;
typedef __attribute__((ext_vector_type(4))) __bf16 bf16x4;
typedef __attribute__((ext_vector_type(2))) __bf16 bf16x2;
typedef __attribute__((ext_vector_type(4))) float f32x4;
typedef __attribute__((ext_vector_type(2))) float f32x2;
typedef __attribute__((ext_vector_type(4))) unsigned u32x4;
typedef __attribute__((ext_vector_type(2))) unsigned u32x2;
typedef __amdgpu_buffer_rsrc_t rsrc_t;
#define AS3 __attribute__((address_space(3)))
#define DEV __device__ __forceinline__
constexpr int S = 2048, D = 640, DF = 1728, RT = 228;
constexpr u32 WS_F0 = 1024, WS_F2 = 2048, WS_F1 = 4096, WS_ST = 16384;
constexpr u32 WS_KV = 64u << 10, WS_Y = WS_KV + S * D * 2, WS_WD = WS_Y + S * D * 2, WS_H = WS_WD + DF * D * 2, WS_SIZE = 20u << 20;
constexpr u32 XS = 29696, WO = 3 * XS, WSZ = 16384, STG = WO + 4 * WSZ, FLG = STG + 8192, ONES = FLG + 256, CSX = FLG + 512;
constexpr u32 L_FULL = FLG, L_CNT0 = FLG + 8, L_P0CNT = FLG + 12, L_FREE = FLG + 16, L_P0ALL = FLG + 32, L_EPI = FLG + 36, L_IMG = FLG + 40,
              L_HCNT = FLG + 52, L_YCNT = FLG + 56, L_HRDY = FLG + 60, L_WDL = FLG + 64, L_BD = FLG + 80, L_AD = FLG + 84, L_YRDY = FLG + 88, L_TAG = FLG + 92,
              L_CS = FLG + 96, L_AS = FLG + 104, L_TAGOK = FLG + 108;
constexpr u32 EPI = STG;
constexpr u32 IMG_U0 = 4160, IMG_U1 = 4160 + 5472, IMGSZ = 4160 + 5472 + 5440;
constexpr u32 IMG = 2 * XS, YST = IMG + 3 * IMGSZ, YT = 40960, RB = 51200;
constexpr u32 LDS_BYTES = 163840;
constexpr u32 OOB = 0x7fffffffu;

DEV u32 rfl(u32 v) { return __builtin_amdgcn_readfirstlane(v); }
DEV rsrc_t mkrs(const void* p, u32 bytes) { return __builtin_amdgcn_make_buffer_rsrc((void*)p, 0, bytes, 0x00020000); }
DEV void dma16(u32 m0v, u32 voff, rsrc_t rs, u32 soff) {
    asm volatile("s_mov_b32 m0, %0\n\ts_nop 0\n\tbuffer_load_dwordx4 %1, %2, %3 offen lds" ::"s"(m0v), "v"(voff), "s"(rs), "s"(soff) : "memory");
}
DEV void dma16n(u32 m0v, u32 voff, rsrc_t rs, u32 soff) {
    asm volatile("s_mov_b32 m0, %0\n\ts_nop 0\n\tbuffer_load_dwordx4 %1, %2, %3 offen nt lds" ::"s"(m0v), "v"(voff), "s"(rs), "s"(soff) : "memory");
}
DEV void dma16c(u32 m0v, u32 voff, rsrc_t rs, u32 soff) {
    asm volatile("s_mov_b32 m0, %0\n\ts_nop 0\n\tbuffer_load_dwordx4 %1, %2, %3 offen sc1 lds" ::"s"(m0v), "v"(voff), "s"(rs), "s"(soff) : "memory");
}
DEV void st128(u32x4 v, u32 voff, rsrc_t rs, u32 soff) {
    asm volatile("buffer_store_dwordx4 %0, %1, %2, %3 offen sc1\n\ts_nop 1" ::"v"(v), "v"(voff), "s"(rs), "s"(soff) : "memory");
}
DEV void st64(u32x2 v, u32 voff, rsrc_t rs, u32 soff) {
    asm volatile("buffer_store_dwordx2 %0, %1, %2, %3 offen sc1\n\ts_nop 1" ::"v"(v), "v"(voff), "s"(rs), "s"(soff) : "memory");
}
DEV void st32(u32 v, u32 voff, rsrc_t rs) { __builtin_amdgcn_raw_buffer_store_b32(v, rs, voff, 0, 16); }
DEV u32x4 ld128(rsrc_t rs, u32 voff, u32 soff) { return __builtin_amdgcn_raw_buffer_load_b128(rs, voff, soff, 0); }
DEV u32x4 ld128n(rsrc_t rs, u32 voff, u32 soff) { return __builtin_amdgcn_raw_buffer_load_b128(rs, voff, soff, 2); }
DEV u32x4 ld128c(rsrc_t rs, u32 voff, u32 soff) { return __builtin_amdgcn_raw_buffer_load_b128(rs, voff, soff, 16); }
DEV u32x2 ld64(rsrc_t rs, u32 voff, u32 soff) { return __builtin_amdgcn_raw_buffer_load_b64(rs, voff, soff, 0); }
DEV bf16x8 lds128(u32 a) { return *(const AS3 bf16x8*)(uintptr_t)a; }
DEV f32x4 lds128f(u32 a) { return *(const AS3 f32x4*)(uintptr_t)a; }
DEV u32x4 lds128u(u32 a) { return *(const AS3 u32x4*)(uintptr_t)a; }
DEV u32 lds32(u32 a) { return *(const AS3 u32*)(uintptr_t)a; }
DEV void sts128(u32 a, u32x4 v) { *(AS3 u32x4*)(uintptr_t)a = v; }
DEV void sts128f(u32 a, f32x4 v) { *(AS3 f32x4*)(uintptr_t)a = v; }
DEV void sts32(u32 a, u32 v) { *(AS3 u32*)(uintptr_t)a = v; }
DEV bf16x4 ldstr(u32 a) { return __builtin_amdgcn_ds_read_tr16_b64_v4bf16((AS3 bf16x4*)(uintptr_t)a); }
DEV u32x2 ldstru(u32 a) { return __builtin_bit_cast(u32x2, ldstr(a)); }
DEV bf16x8 cat(bf16x4 a, bf16x4 b) { return __builtin_shufflevector(a, b, 0, 1, 2, 3, 4, 5, 6, 7); }
DEV bf16x8 asbf(u32x4 v) { return __builtin_bit_cast(bf16x8, v); }
DEV u32 pk2(float a, float b) { f32x2 f = {a, b}; return __builtin_bit_cast(u32, __builtin_convertvector(f, bf16x2)); }
DEV float bfr(float a) { return __uint_as_float(pk2(a, 0.f) << 16); }
DEV float blo(u32 v) { return __uint_as_float(v << 16); }
DEV float bhi(u32 v) { return __uint_as_float(v & 0xffff0000u); }
#define WAITV(n) asm volatile("s_waitcnt vmcnt(" #n ")" ::: "memory")
#define WAITL() asm volatile("s_waitcnt lgkmcnt(0)" ::: "memory")
#define BARL() asm volatile("s_waitcnt lgkmcnt(0)\n\ts_barrier" ::: "memory")
#define PRIO(n) __builtin_amdgcn_s_setprio(n)
#define SB() __builtin_amdgcn_sched_barrier(0)
#define MFMA16(a, b, c) __builtin_amdgcn_mfma_f32_16x16x32_bf16(a, b, c, 0, 0, 0)
DEV u32 ldsflag1(u32 a) { return *(volatile const AS3 u32*)(uintptr_t)a; }
DEV void ldsflagw(u32 a, u32 v) { *(volatile AS3 u32*)(uintptr_t)a = v; }
DEV u32 lds_add(u32 a) {
    u32 r, one = 1;
    asm volatile("s_waitcnt lgkmcnt(0)\n\ts_mov_b64 exec, 1\n\tds_add_rtn_u32 %0, %1, %2\n\ts_mov_b64 exec, -1\n\ts_waitcnt lgkmcnt(0)" : "=&v"(r) : "v"(a), "v"(one) : "memory");
    return rfl(r);
}
DEV void lds_inc(u32 a) {
    u32 one = 1;
    asm volatile("s_mov_b64 exec, 1\n\tds_add_u32 %0, %1\n\ts_mov_b64 exec, -1" ::"v"(a), "v"(one) : "memory");
}
DEV void lds_wait_ge(u32 have, u32 need, u32 vaddr) {
    u32 vt, cn;
    asm volatile(
        "v_cmp_gt_u32 vcc, %3, %2\n\t"
        "s_cbranch_vccnz Lslow%=\n"
        "Ldone%=:\n\t"
        ".subsection 1\n"
        "Lslow%=:\n\t"
        "s_mov_b32 %1, 0x40000\n"
        "Lloop%=:\n\t"
        "ds_read_b32 %0, %4\n\t"
        "s_sub_u32 %1, %1, 1\n\t"
        "s_cbranch_scc1 Ldone%=\n\t"
        "s_waitcnt lgkmcnt(0)\n\t"
        "v_cmp_gt_u32 vcc, %3, %0\n\t"
        "s_cbranch_vccnz Lloop%=\n\t"
        "s_branch Ldone%=\n\t"
        ".subsection 0\n"
        : "=&v"(vt), "=&s"(cn) : "v"(have), "v"(need), "v"(vaddr) : "memory", "scc", "vcc");
}
DEV void lds_spin(u32 vaddr, u32 need) {
    u32 vt, cn;
    asm volatile(
        "s_mov_b32 %1, 0x40000\n"
        "Lsp%=:\n\t"
        "ds_read_b32 %0, %2\n\t"
        "s_waitcnt lgkmcnt(0)\n\t"
        "v_cmp_gt_u32 vcc, %3, %0\n\t"
        "s_cbranch_vccz Lse%=\n\t"
        "s_sleep 1\n\t"
        "s_sub_u32 %1, %1, 1\n\t"
        "s_cbranch_scc0 Lsp%=\n"
        "Lse%=:"
        : "=&v"(vt), "=&s"(cn) : "v"(vaddr), "s"(need) : "memory", "scc", "vcc");
}
DEV void g_poll1(rsrc_t rs, u32 voff, u32 tag) {
    u32 vt, cn;
    asm volatile(
        "s_mov_b32 %1, 0x8000\n"
        "Lgp%=:\n\t"
        "buffer_load_dword %0, %2, %3, 0 offen sc1\n\t"
        "s_waitcnt vmcnt(0)\n\t"
        "v_cmp_ne_u32 vcc, %4, %0\n\t"
        "s_cbranch_vccz Lge%=\n\t"
        "s_sleep 2\n\t"
        "s_sub_u32 %1, %1, 1\n\t"
        "s_cbranch_scc0 Lgp%=\n"
        "Lge%=:"
        : "=&v"(vt), "=&s"(cn) : "v"(voff), "s"(rs), "s"(tag) : "memory", "scc", "vcc");
}

#define TS2(i)
struct TSt {};
#define TS(i)
struct Args { const u16 *x, *k, *v, *sk, *hk, *dv, *wg, *wu, *wd; u16* out; char* ws; };

DEV void kv_tile(const Args& A, rsrc_t rw, int tile, int lane) {
    const int rtile = tile / 10, cg = tile - rtile * 10, r0 = rtile * 64 + (lane >> 3) * 8;
    rsrc_t rk = mkrs(A.k, S * D * 2), rv = mkrs(A.v, S * D * 2), rs = mkrs(A.sk, D * 8);
    const u32 vo = r0 * (D * 2) + cg * 128 + (lane & 7) * 16, vp = r0 ? vo - 3 * D * 2 : OOB;
    u32x4 kk[11], vv[8], w[4];
#pragma unroll
    for (int i = 0; i < 4; ++i) w[i] = ld128(rs, cg * 512 + (lane & 7) * 64, i * 16);
#pragma unroll
    for (int i = 0; i < 11; ++i) kk[i] = i < 3 ? ld128n(rk, vp, i * D * 2) : ld128n(rk, vo, (i - 3) * D * 2);
#pragma unroll
    for (int i = 0; i < 8; ++i) vv[i] = ld128n(rv, vo, i * D * 2);
    const u32 so = (lane & 7) * 8 * 4096 + (2040 - r0) * 2;
#pragma unroll
    for (int c = 0; c < 8; ++c) {
        const u32 wa = w[c >> 1][(c & 1) * 2], wb = w[c >> 1][(c & 1) * 2 + 1];
        const float w0 = blo(wa), w1 = bhi(wa), w2 = blo(wb), w3 = bhi(wb);
        float kf[11], o[8];
#pragma unroll
        for (int i = 0; i < 11; ++i) kf[i] = (c & 1) ? bhi(kk[i][c >> 1]) : blo(kk[i][c >> 1]);
#pragma unroll
        for (int i = 0; i < 8; ++i) {
            o[i] = __builtin_fmaf(kf[i + 3], w0, __builtin_fmaf(kf[i + 2], w1, __builtin_fmaf(kf[i + 1], w2, kf[i] * w3))) * ((c & 1) ? bhi(vv[i][c >> 1]) : blo(vv[i][c >> 1]));
            asm("" : "+v"(o[i]));
        }
        st128(u32x4{pk2(o[7], o[6]), pk2(o[5], o[4]), pk2(o[3], o[2]), pk2(o[1], o[0])}, so, rw, WS_KV + (cg * 64 + c) * 4096);
    }
}
DEV void wd_issue(const Args& A, int pu, u32 lb, int lane) {
    const int s = pu / 5, l2 = pu - s * 5;
    rsrc_t rd = mkrs(A.wd, DF * D * 2);
    const u32 vo = (32 * s + (lane >> 3)) * (D * 2) + l2 * 256 + (lane & 7) * 16;
#pragma unroll
    for (int i = 0; i < 8; ++i) dma16n(lb + i * 1024, vo, rd, (i & 3) * 8 * D * 2 + (i >> 2) * 128);
}
DEV void wd_finish(rsrc_t rw, int pu, u32 lb, int lane) {
    const int s = pu / 5, l2 = pu - s * 5;
    const u32 ta = lb + (lane >> 2) * 128 + (lane & 3) * 8;
#pragma unroll
    for (int f = 0; f < 8; ++f) {
        u32x2 a = ldstru(ta + (f & 3) * 32 + (f >> 2) * 4096), b = ldstru(ta + (f & 3) * 32 + (f >> 2) * 4096 + 2048);
        st128(u32x4{a[0], a[1], b[0], b[1]}, lane * 16, rw, WS_WD + ((8 * l2 + f) * 54 + s) * 1024);
    }
}
template <int CTRL> DEV float dppadd(float v) { return v + __uint_as_float(__builtin_amdgcn_update_dpp(0u, __float_as_uint(v), CTRL, 0xf, 0xf, true)); }
DEV float red8(float s) { s = dppadd<0xB1>(s); s = dppadd<0x4E>(s); return dppadd<0x141>(s); }
DEV void row_stats(const u32x4 (&v)[10], rsrc_t rw, int un, u32 tag, int lane) {
    float s[2] = {0.f, 0.f};
#pragma unroll
    for (int i = 0; i < 10; ++i)
#pragma unroll
        for (int j = 0; j < 4; ++j) { s[0] += blo(v[i][j]); asm("" : "+v"(s[0])); s[1] += bhi(v[i][j]); asm("" : "+v"(s[1])); }
    const float mean = red8(s[0] + s[1]) * (1.f / D);
    s[0] = s[1] = 0.f;
#pragma unroll
    for (int i = 0; i < 10; ++i)
#pragma unroll
        for (int j = 0; j < 4; ++j) {
            float a = blo(v[i][j]) - mean; asm("" : "+v"(a)); s[0] = __builtin_fmaf(a, a, s[0]);
            float b = bhi(v[i][j]) - mean; asm("" : "+v"(b)); s[1] = __builtin_fmaf(b, b, s[1]);
        }
    const float var = red8(s[0] + s[1]) * (1.f / D);
    const float den = __builtin_sqrtf(bfr(bfr(var) + 1e-5f));
    st64(u32x2{pk2(mean, den), tag}, (lane & 7) ? OOB : WS_ST + (un * 8 + (lane >> 3)) * 8, rw, 0);
}
DEV void p0_gather(rsrc_t rw, int g0, u32 tag, int lane) {
    u32x4 sv[2];
    const u32 o0 = WS_ST + min(g0 + lane * 2, S - 2) * 8, o1 = WS_ST + min(g0 + 128 + lane * 2, S - 2) * 8;
#pragma unroll 1
    for (int it = 0; it < 0x4000; ++it) {
        asm volatile("" ::: "memory");
        const u32x4 f = ld128c(rw, WS_F0 + lane * 16, 0);
        sv[0] = ld128c(rw, o0, 0); sv[1] = ld128c(rw, o1, 0);
        const u32 bad = (f[0] ^ tag) | (f[1] ^ tag) | (f[2] ^ tag) | (f[3] ^ tag) | (sv[0][1] ^ tag) | (sv[0][3] ^ tag) | (sv[1][1] ^ tag) | (sv[1][3] ^ tag);
        if (__builtin_amdgcn_ballot_w64(bad != 0u) == 0) break;
        __builtin_amdgcn_s_sleep(8);
    }
    asm volatile("" ::: "memory");
    const float K1 = -2.f * 1.4426950408889634f * 0.7978845608028654f;
#pragma unroll
    for (int i = 0; i < 2; ++i)
#pragma unroll
        for (int j = 0; j < 2; ++j) {
            const float m = blo(sv[i][2 * j]), den = bhi(sv[i][2 * j]);
            const float r = __builtin_amdgcn_rcpf(den);
            sts128f(EPI + (i * 128 + lane * 2 + j) * 16, f32x4{-m, K1 * r, K1 * 0.044715f * r * r * r, den * den});
        }
}

DEV int wrapn(int v, int n) { return v >= n ? v - n : v; }
DEV int g2_step(int off, int w, int i) { return wrapn(off + (i < 6 ? 8 * i + w : 48 + 4 * (i - 6) + w), 54); }
DEV u32 a_area(int w) { return w < 2 ? w * 20480 : 112896 + (w - 2) * 20480; }

DEV void g1_compute(bool busy, int c, int lane, rsrc_t rw, int rt, int grp, bf16x8 (&wr)[4][5], u32 wsrc, int off, TSt& T) {
    const int wi = c >> 1, wj = c & 1;
    const int tok = lane & 15, kq = lane >> 4;
    u32 xa[2], xe[2], wa[2];
    int xm = tok < 8 ? 1 : 0;
    {
        int q = (lane >> 2) & 3, p = lane & 3;
        int f = ((q >> 1) & 1) | ((kq & 1) << 1);
#pragma unroll
        for (int e = 0; e < 2; ++e) {
            wa[e] = WO + 2 * WSZ + (8 * kq + q) * 128 + (((2 * wj + (wi ^ e)) ^ f) << 5) + p * 8;
            u32 t = XS + tok * 128 + (((4 * e + kq) ^ (tok >> 1)) << 4);
            xa[e] = t + wi * 7 * 2048; xe[e] = tok < 8 ? t + 14 * 2048 : ONES;
        }
    }
    f32x4 acc[8][4];
#pragma unroll
    for (int b = 0; b < 8; ++b)
#pragma unroll
        for (int u = 0; u < 4; ++u) acc[b][u] = f32x4{0.f, 0.f, 0.f, 0.f};
    bf16x8 xf[2][8], wf[2][4];
    auto ldf = [&](int ks) {
#pragma unroll
        for (int u = 0; u < 4; ++u) { u32 a = wa[u & 1] + ks * 4096 + (u >> 1) * 8192; wf[ks][u] = cat(ldstr(a), ldstr(a + 512)); }
#pragma unroll
        for (int b = 0; b < 7; ++b) xf[ks][b] = lds128(xa[ks] + b * 2048);
        xf[ks][7] = lds128(xe[ks]);
    };
    auto mm7 = [&](int ks) {
        acc[7][0] = MFMA16(wf[ks][0], xf[ks][7], acc[7][0]);
        acc[7][2] = MFMA16(wf[ks][2], xf[ks][7], acc[7][2]);
    };
    auto mm = [&](int ks, int b0, int b1) {
#pragma unroll
        for (int b = b0; b < b1; ++b)
#pragma unroll
            for (int u = 0; u < 4; ++u) acc[b][u] = MFMA16(wf[ks][u], xf[ks][b], acc[b][u]);
    };
    auto pin = [&]() {
#pragma unroll
        for (int b = 0; b < 7; ++b) asm volatile("" : "+v"(acc[b][0]), "+v"(acc[b][1]), "+v"(acc[b][2]), "+v"(acc[b][3]));
        asm volatile("" : "+v"(acc[7][0]), "+v"(acc[7][2]));
        SB();
    };
    if (busy) {
    lds_spin(L_CNT0, 4);
    TS(1);
    u32 fa = L_FULL + (lane & 1) * 4, fw = L_FREE + c * 4, cnt = 1, need = 2 + (lane & 1);
    asm volatile("" : "+v"(fa), "+v"(fw), "+v"(cnt), "+v"(need), "+v"(xm));
    int xs = 1, wsl = 2;
    u32 have;
    auto halfA = [&]() {
        ldf(1);
        ldsflagw(fw, cnt);
        have = ldsflag1(fa);
        const int dx = xs == 2 ? -2 * (int)XS : (int)XS, dw = wsl == 3 ? -3 * (int)WSZ : (int)WSZ;
        xs = xs == 2 ? 0 : xs + 1; wsl = (wsl + 1) & 3;
        mm(0, 0, 7); mm7(0);
#pragma unroll
        for (int e = 0; e < 2; ++e) { xa[e] += dx; xe[e] += __mul24(xm, dx); wa[e] += dw; }
        cnt += 1;
#pragma unroll
        for (int i = 0; i < 16; ++i) { __builtin_amdgcn_sched_group_barrier(0x008, 1, 0); __builtin_amdgcn_sched_group_barrier(0x100, 1, 0); }
        __builtin_amdgcn_sched_group_barrier(0x008, 1, 0); __builtin_amdgcn_sched_group_barrier(0x200, 1, 0);
        __builtin_amdgcn_sched_group_barrier(0x008, 1, 0); __builtin_amdgcn_sched_group_barrier(0x100, 1, 0);
#pragma unroll
        for (int i = 0; i < 8; ++i) { __builtin_amdgcn_sched_group_barrier(0x008, 1, 0); __builtin_amdgcn_sched_group_barrier(0x002, 1, 0); }
        __builtin_amdgcn_sched_group_barrier(0x008, 4, 0);
        asm volatile("" : "+v"(xa[0]), "+v"(xa[1]), "+v"(xe[0]), "+v"(xe[1]), "+v"(wa[0]), "+v"(wa[1]), "+v"(cnt), "+v"(have));
        pin();
    };
    ldf(0);
    pin();
    halfA();
#pragma unroll 1
    for (int st = 0; st < 9; ++st) {
        lds_wait_ge(have, need, fa);
        need = (need + 2) ^ 1;
        ldf(0);
        mm(1, 0, 7); mm7(1);
#pragma unroll
        for (int i = 0; i < 16; ++i) { __builtin_amdgcn_sched_group_barrier(0x008, 1, 1); __builtin_amdgcn_sched_group_barrier(0x100, 1, 1); }
        __builtin_amdgcn_sched_group_barrier(0x008, 14, 1);
        pin();
        halfA();
    }
    mm7(1); mm(1, 0, 2);
    SB();
    if (tok == 8) {
        sts128f(CSX + (2 * wj + wi) * 64 + kq * 16, acc[7][0]);
        sts128f(CSX + 256 + (2 * wj + wi) * 64 + kq * 16, acc[7][2]);
    }
    lds_inc(L_CS + wj * 4);
    SB();
    mm(1, 2, 7);
    pin();
    }
    TS(2);
    lds_spin(lane & 1 ? L_EPI : L_CS + wj * 4, 2);
    TS(3);
    f32x4 cs[4];
#pragma unroll
    for (int u = 0; u < 4; ++u) cs[u] = lds128f(CSX + (u >> 1) * 256 + (2 * wj + (wi ^ (u & 1))) * 64 + kq * 16);
    auto act2 = [&](const f32x4& e, const f32x4& g, const f32x4& u, const f32x4& cg, const f32x4& cu, int i) {
        const f32x2 e0 = {e[0], e[0]}, e1 = {e[1], e[1]}, e2 = {e[2], e[2]}, e3 = {e[3], e[3]};
        const f32x2 a = __builtin_elementwise_fma(e0, f32x2{cg[i], cg[i + 1]}, f32x2{g[i], g[i + 1]});
        const f32x2 b = __builtin_elementwise_fma(e0, f32x2{cu[i], cu[i + 1]}, f32x2{u[i], u[i + 1]});
        const f32x2 z = a * __builtin_elementwise_fma(a * a, e2, e1);
        const f32x2 ex = {__builtin_amdgcn_exp2f(z[0]), __builtin_amdgcn_exp2f(z[1])};
        const f32x2 d = __builtin_elementwise_fma(ex, e3, e3);
        const f32x2 r = (a * b) * f32x2{__builtin_amdgcn_rcpf(d[0]), __builtin_amdgcn_rcpf(d[1])};
        return pk2(r[0], r[1]);
    };
    const int gl = ((rt * RT) & 15) + tok;
    const u32 hv = busy ? (gl >> 4) * (54 * 1024) + (kq * 16 + (gl & 15)) * 16 : OOB;
    const u32 hbase = WS_H + ((((rt * RT) >> 4) + wi * 7) * 54 + grp * 2 + wj) * 1024;
#pragma unroll
    for (int b = 0; b < 7; ++b) {
#pragma unroll
        for (int j = 3 * b; j < 3 * b + 3; ++j)
            if (j < 20) wr[j / 5][j % 5] = asbf(ld128c(rw, lane * 16, wsrc + ((j % 5) * 54 + g2_step(off, c, j / 5)) * 1024));
        SB();
        f32x4 e = lds128f(EPI + ((wi * 7 + b) * 16 + tok) * 16);
        u32 o[4];
#pragma unroll
        for (int h = 0; h < 2; ++h) { o[2 * h] = act2(e, acc[b][h], acc[b][2 + h], cs[h], cs[2 + h], 0); o[2 * h + 1] = act2(e, acc[b][h], acc[b][2 + h], cs[h], cs[2 + h], 2); }
        u32x4 ov = wi ? u32x4{o[2], o[3], o[0], o[1]} : u32x4{o[0], o[1], o[2], o[3]};
        st128(ov, hv, rw, hbase + b * 54 * 1024);
    }
    if (rt < 8) {
        f32x4 e = lds128f(EPI + (14 * 16 + tok) * 16);
        u32x2 ov = {act2(e, acc[7][0], acc[7][2], cs[0], cs[2], 0), act2(e, acc[7][0], acc[7][2], cs[0], cs[2], 2)};
        st64(ov, tok < 4 ? hv : OOB, rw, hbase + (14 - wi * 7) * 54 * 1024 + wi * 8);
    }
}
struct G1Src { rsrc_t rx, rg, ru; u32 xv0, xv1, xl, wv0, wv1; int ro; };
DEV void ld_x(const G1Src& g, int s, int p0, int dp) {
    const u32 so = wrapn(s + 1 - (s >= 5 ? 6 : s >= 2 ? 3 : 0), 3) * XS, ks = wrapn(s + g.ro, 10) * 128;
#pragma unroll
    for (int p = p0; p < 29; p += dp) dma16(so + p * 1024, p == 28 ? g.xl : (p & 1) ? g.xv1 : g.xv0, g.rx, ks + p * 8 * D * 2);
}
DEV void ld_w(const G1Src& g, int s, int p0, int dp) {
    const u32 so = WO + ((s + 2) & 3) * WSZ, ks = wrapn(s + g.ro, 10) * (64 * DF * 2);
#pragma unroll
    for (int p = p0; p < 16; p += dp) dma16(so + p * 1024, (p & 1) ? g.wv1 : g.wv0, (p >> 3) ? g.ru : g.rg, ks + (p & 7) * 8 * DF * 2);
}
DEV void g1_loader(int e, const G1Src& g, int lane, rsrc_t rw) {
    const u32 fr = L_FREE + (lane & 3) * 4, fu = L_FULL + e * 4;
    ld_w(g, e ? 1 : 3, 0, 1);
#pragma unroll 1
    for (int s = e ? 1 : 2; s < 10; s += 2) {
        if (s > 2) lds_spin(fr, s - 2);
        TS2(s);
        ld_x(g, s, 0, 1);
        if (s != 2 && s != 9) {
            ld_w(g, s + 1, 0, 1);
            WAITV(16);
            ldsflagw(fu, 2 * s + 1);
        }
        WAITV(0);
        ldsflagw(fu, 2 * s + 2);
        TS2(s + 1);
    }
}

DEV void conv_build(const u32x4 (&v)[4], u32 ib, int lane) {
    const u32 ua = ib + (lane >> 4) * 320 + (lane & 15) * 16;
#pragma unroll
    for (int i = 0; i < 4; ++i) {
        u32 sel = lane == 0 ? (i < 3 ? v[i < 3 ? i + 1 : 0][0] : 0u) : v[i][0];
        u32 nx = __builtin_amdgcn_ds_bpermute(((lane + 1) & 63) * 4, sel);
        sts128(ua + IMG_U0 + i * 1280, v[i]);
        sts128(ua + IMG_U1 + i * 1280, u32x4{__builtin_amdgcn_alignbit(v[i][1], v[i][0], 16), __builtin_amdgcn_alignbit(v[i][2], v[i][1], 16),
                                             __builtin_amdgcn_alignbit(v[i][3], v[i][2], 16), __builtin_amdgcn_alignbit(nx, v[i][3], 16)});
    }
    if (lane < 36) sts128(lane < 32 ? ib + (lane < 16 ? IMG_U0 : IMG_U1) + 5120 + (lane & 15) * 16 : ib + (lane - 32) * 16, u32x4{0u, 0u, 0u, 0u});
}
DEV bf16x8 ldsF(u32 a) { return asbf(u32x4{lds32(a), lds32(a + 4), lds32(a + 8), lds32(a + 12)}); }
struct WdDma { int i, n, ok, f, sl, off, w; u32 lds, src; };
DEV void wd_dma_piece(WdDma& w, rsrc_t rw, int lane) {
    dma16c(w.lds + w.i * 1024, lane * 16, rw, w.src + (w.f * 54 + g2_step(w.off, w.w, w.sl)) * 1024);
    ++w.i; ++w.f;
    if (w.f == 5) { w.f = 0; ++w.sl; }
}
DEV void conv_task(const Args& A, rsrc_t rw, int ch, int g, u32 ib, u32 yst, int lane, WdDma& wd) {
    const int a = lane & 15, q = lane >> 4;
    u32 ha = ib + 4 + 4 * a + 16 * q;
    const u32 ub = ib + ((a & 1) ? IMG_U0 : IMG_U1) + 16 * q;
    u32 ua = ub + (15 - 8 * g - (a >> 1)) * 320;
    const u32 uz = ub + 5120;
    const float dv = blo(A.dv[ch]);
    f32x4 acc[4];
    bf16x8 f[4], u[4];
#pragma unroll
    for (int w = 0; w < 4; ++w) { acc[w] = f32x4{0.f, 0.f, 0.f, 0.f}; f[w] = asbf(u32x4{0u, 0u, 0u, 0u}); }
    f[0] = ldsF(ha);
    u[0] = lds128(ua); u[1] = lds128(ua + 64);
    const int nit = 8 * (g + 1);
#pragma unroll 1
    for (int it = 0; it < nit; ++it) {
        const u32 un = min(ua + 320, uz);
#pragma unroll
        for (int t = 0; t < 4; ++t) {
            acc[0] = MFMA16(u[t], f[(t + 1) & 3], acc[0]);
            u[(t + 2) & 3] = lds128(t < 2 ? ua + (t + 2) * 64 : un + (t - 2) * 64);
            f[(t + 1) & 3] = ldsF(ha + (t + 1) * 64);
            acc[1] = MFMA16(u[t], f[(t + 2) & 3], acc[1]);
            acc[2] = MFMA16(u[t], f[(t + 3) & 3], acc[2]);
            acc[3] = MFMA16(u[t], f[t], acc[3]);
            SB();
        }
        ua = un; ha += 256;
        if (wd.i < wd.n) {
            if (!wd.ok) wd.ok = __builtin_amdgcn_ballot_w64(ldsflag1(L_FREE + (lane & 3) * 4) < 10u) == 0;
            if (wd.ok) { wd_dma_piece(wd, rw, lane); if (wd.i < wd.n) wd_dma_piece(wd, rw, lane); }
        }
    }
#pragma unroll
    for (int w = 0; w < 4; ++w)
#pragma unroll
        for (int ih = 0; ih < 2; ++ih) {
            const int sl = 32 * w + 2 * a + 128 * (2 * q + ih);
            const int e = 2046 - 1024 * g - sl;
            const u32 uu = lds32(ib + IMG_U0 + (e >> 7) * 320 + (e & 127) * 2);
            sts32(yst + sl * 2, pk2(bfr(acc[w][2 * ih]) + bfr(bhi(uu) * dv), bfr(acc[w][2 * ih + 1]) + bfr(blo(uu) * dv)));
        }
    asm volatile("" ::: "memory");
#pragma unroll
    for (int i = 0; i < 2; ++i) st128(lds128u(yst + i * 1024 + lane * 16), lane * 16, rw, WS_Y + ch * 4096 + g * 2048 + i * 1024);
}

DEV void g2_b(const Args& A, rsrc_t rw, const bf16x8 (&wr)[6][5], int w, int off, int blk, int cgp, int lane, TSt& T) {
    f32x4 acc[4][5];
#pragma unroll
    for (int t = 0; t < 4; ++t)
#pragma unroll
        for (int f = 0; f < 5; ++f) acc[t][f] = f32x4{0.f, 0.f, 0.f, 0.f};
    bf16x8 hf[6][4];
    auto ldh = [&](int i) {
#pragma unroll
        for (int t = 0; t < 4; ++t) hf[i][t] = asbf(ld128c(rw, lane * 16, WS_H + ((blk + ((t + 1 + w) & 3)) * 54 + g2_step(off, w + 4, i)) * 1024));
    };
    ldh(0); ldh(1);
    lds_spin(L_YCNT, 4);
#pragma unroll
    for (int i = 0; i < 6; ++i) {
#pragma unroll
        for (int t = 0; t < 4; ++t) {
#pragma unroll
            for (int f = 0; f < 5; ++f) acc[t][f] = MFMA16(wr[i][f], hf[i][t], acc[t][f]);
            if (i == 5 && t > 0) {
#pragma unroll
                for (int f = 0; f < 5; ++f) sts128f(RB + ((((t + w) & 3) * 3 + t - 1) * 5 + f) * 1024 + lane * 16, acc[t - 1][f]);
                SB();
            }
        }
        SB();
        if (i + 2 < 6) ldh(i + 2);
        SB();
    }
    lds_inc(L_BD);
    TS(7);
    lds_spin(L_AS, 4);
#pragma unroll
    for (int f = 0; f < 5; ++f) acc[3][f] += lds128f(a_area(w) + (15 + f) * 1024 + lane * 16);
    TS(8);
    lds_spin(L_BD, 4);
    TS(9);
    rsrc_t ro = mkrs(A.out, S * D * 2);
    const u32 ov = ((blk + w) * 16 + (lane & 15)) * (D * 2) + cgp * 160 + (lane >> 4) * 8;
#pragma unroll
    for (int f = 0; f < 5; ++f) {
#pragma unroll
        for (int j = 0; j < 3; ++j) acc[3][f] += lds128f(RB + ((w * 3 + j) * 5 + f) * 1024 + lane * 16);
        __builtin_amdgcn_raw_buffer_store_b64(u32x2{pk2(acc[3][f][0], acc[3][f][1]), pk2(acc[3][f][2], acc[3][f][3])}, ro, ov + f * 32, 0, 0);
    }
}
DEV void g2_a(const Args& A, rsrc_t rw, const bf16x8 (&wr)[4][5], int w, int off, int blk, int cgp, u32 tag, int lane, TSt& T) {
    f32x4 acc[4][5];
#pragma unroll
    for (int t = 0; t < 4; ++t)
#pragma unroll
        for (int f = 0; f < 5; ++f) acc[t][f] = f32x4{0.f, 0.f, 0.f, 0.f};
    bf16x8 hf[8][4], wl[4][5];
    const u32 la = a_area(w) + lane * 16;
    auto ldh = [&](int i) {
        const u32 vo = (i == 7 && w >= 2) ? OOB : (u32)lane * 16;
#pragma unroll
        for (int t = 0; t < 4; ++t) hf[i][t] = asbf(ld128c(rw, vo, WS_H + ((blk + ((t + 1 + w) & 3)) * 54 + g2_step(off, w, i)) * 1024));
    };
    auto ldw = [&](int i) {
#pragma unroll
        for (int f = 0; f < 5; ++f) wl[i][f] = lds128(la + ((i == 3 && w >= 2 ? 2 : i) * 5 + f) * 1024);
    };
    ldh(0); ldh(1);
#pragma unroll
    for (int i = 0; i < 8; ++i) {
        if (i == 3) lds_spin(L_WDL + w * 4, 1);
        if (i >= 3 && i < 7) ldw(i - 3);
        SB();
#pragma unroll
        for (int t = 0; t < 4; ++t)
#pragma unroll
            for (int f = 0; f < 5; ++f) acc[t][f] = MFMA16(i < 4 ? wr[i & 3][f] : wl[i & 3][f], hf[i][t], acc[t][f]);
        SB();
        if (i + 2 < 8) ldh(i + 2);
        SB();
    }
    TS(7);
    const int row = (blk + w) * 16 + (lane & 15);
    rsrc_t rx = mkrs(A.x, S * D * 2);
    u32x2 xv[5];
#pragma unroll
    for (int f = 0; f < 5; ++f) xv[f] = ld64(rx, row * (D * 2) + cgp * 160 + (lane >> 4) * 8, f * 32);
    const u32 sv = __builtin_amdgcn_raw_buffer_load_b32(rw, WS_ST + row * 8, 0, 16);
    if (w == 3) {
        g_poll1(rw, WS_F2 + cgp * 128 + (lane & 31) * 4, tag);
#pragma unroll
        for (int i = 0; i < 10; ++i) dma16c(YT + i * 1024, (lane >> 3) * 4096 + (lane & 7) * 16, rw, WS_Y + (cgp * 80 + i * 8) * 4096 + blk * 32);
    }
#pragma unroll
    for (int t = 0; t < 3; ++t)
#pragma unroll
        for (int f = 0; f < 5; ++f) sts128f(la + (t * 5 + f) * 1024, acc[t][f]);
    lds_inc(L_AD);
    if (w == 3) { WAITV(0); ldsflagw(L_YRDY, 4u); }
    lds_spin(L_AD, 4);
    TS(8);
#pragma unroll
    for (int j = 0; j < 3; ++j)
#pragma unroll
        for (int f = 0; f < 5; ++f) acc[3][f] += lds128f(a_area((w + 3 - j) & 3) + (j * 5 + f) * 1024 + lane * 16);
    lds_spin(L_YRDY, 4);
    TS(9);
    const float m = blo(sv), r = __builtin_amdgcn_rcpf(bhi(sv));
#pragma unroll
    for (int f = 0; f < 5; ++f) {
        const u32x2 y = ldstru(YT + (16 * f + (lane >> 2)) * 128 + w * 32 + (lane & 3) * 8);
        const float xx[4] = {blo(xv[f][0]), bhi(xv[f][0]), blo(xv[f][1]), bhi(xv[f][1])}, yy[4] = {blo(y[0]), bhi(y[0]), blo(y[1]), bhi(y[1])};
#pragma unroll
        for (int i = 0; i < 4; ++i) acc[3][f][i] += bfr(xx[i] + bfr(bfr(bfr(xx[i] - m) * r) * yy[i]));
        sts128f(la + (15 + f) * 1024, acc[3][f]);
    }
    lds_inc(L_AS);
}

__global__ __launch_bounds__(512, 2) void fused_kernel(Args A) {
    const int b = blockIdx.x, lane = threadIdx.x & 63, wid = rfl(threadIdx.x >> 6);
    const int xcd = b & 7, idx = b >> 3;
    rsrc_t rw = mkrs(A.ws, WS_SIZE);
    TSt T = {};
    TS(0);
    int grp = 0, rt = 0, idle = -1, ro;
    if (xcd < 6) {
        int c = (xcd & 1) * 32 + idx;
        if (c == 63) idle = xcd >> 1;
        grp = c / 3; const int r3 = c - grp * 3; rt = (xcd >> 1) * 3 + r3;
        ro = (grp + 3 * r3) % 10;
    } else {
        int gi;
        if (xcd == 6) { if (idx >= 30) idle = idx - 27; gi = idx / 5; rt = idx - gi * 5; }
        else { if (idx >= 24) idle = idx - 19; gi = idx >> 2; rt = 5 + (idx & 3); }
        grp = 21 + gi;
        ro = 2 * ((gi + rt) % 5);
    }
    const bool busy = idle < 0;
    G1Src g;
    g.rx = mkrs(A.x, S * D * 2); g.rg = mkrs(A.wg, D * DF * 2); g.ru = mkrs(A.wu, D * DF * 2); g.ro = ro;
    { int row = lane >> 3;
      g.xv0 = (rt * RT + row) * (D * 2) + (((lane & 7) ^ (row >> 1)) << 4); g.xv1 = g.xv0 ^ 64; g.xl = row < 4 ? g.xv0 : OOB;
      g.wv0 = row * (DF * 2) + grp * 128 + (((lane & 7) ^ (((row >> 1) & 1) << 1)) << 4); g.wv1 = g.wv0 ^ 64; }
    if (wid < 4) {
        PRIO(3);
        if (busy) {
            if (wid & 1) { g.xv0 = g.xv1; g.wv0 = g.wv1; g.xl = OOB; }
            const u32 ks = ro * 128 + (wid & 1) * 8 * D * 2, kw = ro * (64 * DF * 2) + (wid & 1) * 8 * DF * 2, lo = (wid & 1) * 1024;
            if (wid < 2) {
#pragma unroll
                for (int p = 0; p < 22; p += 2) dma16(XS + lo + p * 1024, g.xv0, g.rx, ks + p * 8 * D * 2);
            } else {
#pragma unroll
                for (int p = 0; p < 16; p += 2) dma16(WO + 2 * WSZ + lo + p * 1024, g.wv0, (p >> 3) ? g.ru : g.rg, kw + (p & 7) * 8 * DF * 2);
#pragma unroll
                for (int p = 22; p < 28; p += 2) dma16(XS + lo + p * 1024, g.xv0, g.rx, ks + p * 8 * D * 2);
                if (wid == 2) dma16(XS + 28 * 1024, g.xl, g.rx, ks + 28 * 8 * D * 2);
            }
        }
    }
    if (threadIdx.x >= 448 && lane < 36) ldsflagw(lane < 32 ? FLG + lane * 4 : ONES + (lane - 32) * 4, lane >= 32 ? 0x3F803F80u : lane == 0 ? 2u : (!busy && ((lane >= 4 && lane < 8) || lane == 24 || lane == 25)) ? 10u : 0u);
    BARL();
    if (busy && wid < 4) { WAITV(0); lds_inc(L_CNT0); }
    if (!busy || wid >= 6) {
        const int q = rt * 27 + grp;
        int kvt = -1, wdp = -1, stu = -1;
        if (busy) { if (wid == 6) kvt = q; else { wdp = q; stu = q + q / 18; } }
        else {
            if (wid < 6) { kvt = 243 + idle * 6 + wid; if (kvt == 320) { kvt = -1; wdp = 269; } }
            else wdp = 243 + 2 * idle + wid - 6;
            if (wid == 7) stu = 19 * idle + 18;
        }
        const u32 lb = busy ? STG : wid * 8192;
        if (kvt >= 0) kv_tile(A, rw, kvt, lane);
        else {
            u32 ep = 0;
            if (wid == 7) ep = __builtin_amdgcn_raw_buffer_load_b32(rw, 0, 0, 16);
            wd_issue(A, wdp, lb, lane);
            if (wid == 7) {
                u32x4 v[10];
#pragma unroll
                for (int i = 0; i < 10; ++i) v[i] = ld128(g.rx, (stu * 8 + (lane >> 3)) * (D * 2) + (lane & 7) * 16, i * 128);
                ep += 0x9E3779B1u;
                ldsflagw(L_TAG, ep);
                row_stats(v, rw, stu, ep, lane);
            }
            WAITV(0);
            wd_finish(rw, wdp, lb, lane);
        }
        WAITV(0);
        if (lds_add(L_P0CNT) == (busy ? 1u : 7u) && lane == 0) st32(ldsflag1(L_TAG), WS_F0 + b * 4, rw);
    }
    const int cgp = idx & 7, jr = idx >> 3, blk = (xcd * 4 + jr) * 4;
    const u32 wsrc = WS_WD + cgp * 5 * 54 * 1024;
    const int w4 = wid & 3, off = wrapn(7 * cgp + 4 * (jr & 1) + 24 * (jr >> 1), 54);
    if (wid < 4) {
        bf16x8 wr[4][5];
        g1_compute(busy, wid, lane, rw, rt, grp, wr, wsrc, off, T);
        TS(4);
        WAITV(0);
        TS(5);
        if (lds_add(L_HCNT) == 3u && lane == 0 && busy) st32(ldsflag1(L_TAG), WS_F1 + (rt * 32 + grp) * 16, rw);
        const u32 tag = rfl(ldsflag1(L_TAG));
        if (wid == 0) {
            const int r = (blk * 16 + (lane < 32 ? 0 : 63)) / RT;
            g_poll1(rw, WS_F1 + (r * 32 + min(lane & 31, 26)) * 16, tag);
            ldsflagw(L_HRDY, 1u);
        } else lds_spin(L_HRDY, 1);
        TS(6);
        g2_a(A, rw, wr, wid, off, blk, cgp, tag, lane, T);
        TS(10);
    } else {
        if (wid < 6) { if (busy) { PRIO(2); g1_loader(wid - 4, g, lane, rw); PRIO(0); } }
        TS(1);
        const u32 fr = L_FREE + (lane & 3) * 4;
        if (wid == 7) {
            const u32 tg = rfl(ldsflag1(L_TAG));
            lds_spin(fr, 5);
            p0_gather(rw, rt * RT, tg, lane);
            ldsflagw(L_P0ALL, 1u);
            ldsflagw(L_EPI, 2u);
            if (b == 0 && lane == 0) st32(tg, 0, rw);
        } else lds_spin(L_P0ALL, 1);
        TS(2);
        const u32 tag = rfl(ldsflag1(L_TAG));
        bf16x8 wr[6][5];
        auto ldw = [&](int i0, int i1) {
#pragma unroll
            for (int j = 5 * i0; j < 5 * i1; ++j) wr[j / 5][j % 5] = asbf(ld128c(rw, lane * 16, wsrc + ((j % 5) * 54 + g2_step(off, wid, j / 5)) * 1024));
        };
        WdDma wd = {0, w4 < 2 ? 20 : 15, 0, 0, 4, off, w4, a_area(w4), wsrc};
        const int pr = (b >> 4) * 8 + xcd, mem = idx & 1;
        const u32 yst = YST + w4 * 2048;
        const int ch = pr * 5 + (mem ? (w4 == 2 ? 2 : w4 + 3) : w4);
        const u32 ib = IMG + w4 * IMGSZ;
        lds_spin(fr, 8);
        if (wid < 7) {
            rsrc_t rh = mkrs(A.hk, D * S * 2);
            u32x4 v[4];
#pragma unroll
            for (int i = 0; i < 4; ++i) v[i] = ld128c(rw, WS_KV + lane * 16, ch * 4096 + i * 1024);
#pragma unroll
            for (int i = 0; i < 4; ++i) dma16n(ib + 64 + i * 1024, lane * 16, rh, ch * 4096 + i * 1024);
            SB();
            ldw(0, 2);
            SB();
            conv_build(v, ib, lane);
            WAITV(10);
            WAITL();
            ldsflagw(L_IMG + w4 * 4, 1u);
        } else ldw(0, 2);
        SB();
        ldw(2, 6);
        SB();
        TS(3);
        const int nt = wid == 7 ? 2 : 1;
#pragma unroll 1
        for (int sl = 0; sl < nt; ++sl) {
            if (wid == 7) lds_spin(L_IMG + sl * 4, 1);
            conv_task(A, rw, wid == 7 ? pr * 5 + mem * 3 + sl : ch, (wid == 7 || (mem && w4 == 2)) ? 0 : 1, wid == 7 ? IMG + sl * IMGSZ : ib, yst, lane, wd);
        }
        TS(4);
        if (wd.i < wd.n) {
            lds_spin(fr, 10);
#pragma unroll 1
            while (wd.i < wd.n) wd_dma_piece(wd, rw, lane);
        }
        WAITV(0);
        TS(5);
        ldsflagw(L_WDL + w4 * 4, 1u);
        if (lds_add(L_YCNT) == 3u && lane == 0) st32(tag, WS_F2 + (pr * 2 + mem) * 4, rw);
        lds_spin(L_HRDY, 1);
        TS(6);
        g2_b(A, rw, wr, w4, off, blk, cgp, lane, T);
        TS(11);
    }
}

extern "C" void kernel_launch(void* const* d_in, const int* in_sizes, int n_in, void* d_out, int out_size, void* d_ws, size_t ws_size, hipStream_t stream) {
    Args A;
    A.x = (const u16*)d_in[0]; A.k = (const u16*)d_in[1]; A.v = (const u16*)d_in[2]; A.sk = (const u16*)d_in[3]; A.hk = (const u16*)d_in[4];
    A.dv = (const u16*)d_in[5]; A.wg = (const u16*)d_in[7]; A.wu = (const u16*)d_in[8]; A.wd = (const u16*)d_in[9];
    A.out = (u16*)d_out; A.ws = (char*)d_ws;
    (void)hipFuncSetAttribute((const void*)fused_kernel, hipFuncAttributeMaxDynamicSharedMemorySize, LDS_BYTES);
    fused_kernel<<<256, 512, LDS_BYTES, stream>>>(A);
}
```

```cpp
#include <hip/hip_runtime.h>
#include <stdint.h>
typedef unsigned short u16;
typedef unsigned int u32;
typedef __attribute__((ext_vector_type(8))) __bf16 bf16x8;
typedef __attribute__((ext_vector_type(4))) __bf16 bf16x4;
typedef __attribute__((ext_vector_type(2))) __bf16 bf16x2;
typedef __attribute__((ext_vector_type(4))) float f32x4;
typedef __attribute__((ext_vector_type(2))) float f32x2;
typedef __attribute__((ext_vector_type(4))) unsigned u32x4;
typedef __attribute__((ext_vector_type(2))) unsigned u32x2;
typedef __amdgpu_buffer_rsrc_t rsrc_t;
#define AS3 __attribute__((address_space(3)))
#define DEV __device__ __forceinline__
constexpr int S = 2048, D = 640, DF = 1728, RT = 228;
constexpr u32 WS_FK = 4096, WS_FW = 16384, WS_F2 = 24576, WS_ST = 32768, WS_F1 = 49152;
constexpr u32 WS_KV = 128u << 10, WS_Y = WS_KV + S * D * 2, WS_WD = WS_Y + S * D * 2, WS_H = WS_WD + DF * D * 2, WS_SIZE = 20u << 20;
constexpr u32 XS = 29696, WO = 3 * XS, WSZ = 16384, STG = WO + 4 * WSZ, FLG = STG + 8192, ONES = FLG + 256, CSX = FLG + 512;
constexpr u32 L_FULL = FLG, L_CNT0 = FLG + 8, L_LDONE = FLG + 12, L_FREE = FLG + 16, L_P0ALL = FLG + 32, L_EPI = FLG + 36, L_IMG = FLG + 40,
              L_HCNT = FLG + 52, L_YCNT = FLG + 56, L_HRDY = FLG + 60, L_BD = FLG + 80, L_AD = FLG + 84, L_YRDY = FLG + 88, L_TAG = FLG + 92,
              L_ISS0 = FLG + 64, L_YOK = FLG + 68, L_CS = FLG + 96, L_AS = FLG + 104, L_TAGOK = FLG + 108;
constexpr u32 EPI = STG;
constexpr u32 IMG_U0 = 4160, IMG_U1 = 4160 + 5472, IMGSZ = 4160 + 5472 + 5440;
constexpr u32 IMG = 2 * XS, YST = IMG + 3 * IMGSZ, YT = 40960, RB = 51200;
constexpr u32 LDS_BYTES = 163840;
constexpr u32 OOB = 0x7fffffffu;
#define YAT 2
#define P0PRIO 0
#define THR 6
#define OUTAUX 0

DEV u32 rfl(u32 v) { return __builtin_amdgcn_readfirstlane(v); }
DEV rsrc_t mkrs(const void* p, u32 bytes) { return __builtin_amdgcn_make_buffer_rsrc((void*)p, 0, bytes, 0x00020000); }
DEV void dma16(u32 m0v, u32 voff, rsrc_t rs, u32 soff) {
    asm volatile("s_mov_b32 m0, %0\n\ts_nop 0\n\tbuffer_load_dwordx4 %1, %2, %3 offen lds" ::"s"(m0v), "v"(voff), "s"(rs), "s"(soff) : "memory");
}
DEV void dma16n(u32 m0v, u32 voff, rsrc_t rs, u32 soff) {
    asm volatile("s_mov_b32 m0, %0\n\ts_nop 0\n\tbuffer_load_dwordx4 %1, %2, %3 offen nt lds" ::"s"(m0v), "v"(voff), "s"(rs), "s"(soff) : "memory");
}
DEV void st128(u32x4 v, u32 voff, rsrc_t rs, u32 soff) {
    asm volatile("buffer_store_dwordx4 %0, %1, %2, %3 offen sc1\n\ts_nop 1" ::"v"(v), "v"(voff), "s"(rs), "s"(soff) : "memory");
}
DEV void st64(u32x2 v, u32 voff, rsrc_t rs, u32 soff) {
    asm volatile("buffer_store_dwordx2 %0, %1, %2, %3 offen sc1\n\ts_nop 1" ::"v"(v), "v"(voff), "s"(rs), "s"(soff) : "memory");
}
DEV void st32(u32 v, u32 voff, rsrc_t rs) { __builtin_amdgcn_raw_buffer_store_b32(v, rs, voff, 0, 16); }
DEV u32x4 ld128(rsrc_t rs, u32 voff, u32 soff) { return __builtin_amdgcn_raw_buffer_load_b128(rs, voff, soff, 0); }
DEV u32x4 ld128n(rsrc_t rs, u32 voff, u32 soff) { return __builtin_amdgcn_raw_buffer_load_b128(rs, voff, soff, 2); }
DEV u32x4 ld128c(rsrc_t rs, u32 voff, u32 soff) { return __builtin_amdgcn_raw_buffer_load_b128(rs, voff, soff, 16); }
DEV u32x2 ld64(rsrc_t rs, u32 voff, u32 soff) { return __builtin_amdgcn_raw_buffer_load_b64(rs, voff, soff, 0); }
DEV bf16x8 lds128(u32 a) { return *(const AS3 bf16x8*)(uintptr_t)a; }
DEV f32x4 lds128f(u32 a) { return *(const AS3 f32x4*)(uintptr_t)a; }
DEV u32x4 lds128u(u32 a) { return *(const AS3 u32x4*)(uintptr_t)a; }
DEV u32 lds32(u32 a) { return *(const AS3 u32*)(uintptr_t)a; }
DEV void sts128(u32 a, u32x4 v) { *(AS3 u32x4*)(uintptr_t)a = v; }
DEV void sts128f(u32 a, f32x4 v) { *(AS3 f32x4*)(uintptr_t)a = v; }
DEV void sts32(u32 a, u32 v) { *(AS3 u32*)(uintptr_t)a = v; }
DEV bf16x4 ldstr(u32 a) { return __builtin_amdgcn_ds_read_tr16_b64_v4bf16((AS3 bf16x4*)(uintptr_t)a); }
DEV u32x2 ldstru(u32 a) { return __builtin_bit_cast(u32x2, ldstr(a)); }
DEV bf16x8 cat(bf16x4 a, bf16x4 b) { return __builtin_shufflevector(a, b, 0, 1, 2, 3, 4, 5, 6, 7); }
DEV bf16x8 asbf(u32x4 v) { return __builtin_bit_cast(bf16x8, v); }
DEV u32 pk2(float a, float b) { f32x2 f = {a, b}; return __builtin_bit_cast(u32, __builtin_convertvector(f, bf16x2)); }
DEV float bfr(float a) { return __uint_as_float(pk2(a, 0.f) << 16); }
DEV float blo(u32 v) { return __uint_as_float(v << 16); }
DEV float bhi(u32 v) { return __uint_as_float(v & 0xffff0000u); }
#define WAITV(n) asm volatile("s_waitcnt vmcnt(" #n ")" ::: "memory")
#define WAITL() asm volatile("s_waitcnt lgkmcnt(0)" ::: "memory")
#define BARL() asm volatile("s_waitcnt lgkmcnt(0)\n\ts_barrier" ::: "memory")
#define PRIO(n) __builtin_amdgcn_s_setprio(n)
#define SB() __builtin_amdgcn_sched_barrier(0)
#define MFMA16(a, b, c) __builtin_amdgcn_mfma_f32_16x16x32_bf16(a, b, c, 0, 0, 0)
DEV u32 ldsflag1(u32 a) { return *(volatile const AS3 u32*)(uintptr_t)a; }
DEV void ldsflagw(u32 a, u32 v) { *(volatile AS3 u32*)(uintptr_t)a = v; }
DEV u32 lds_add(u32 a) {
    u32 r, one = 1;
    asm volatile("s_waitcnt lgkmcnt(0)\n\ts_mov_b64 exec, 1\n\tds_add_rtn_u32 %0, %1, %2\n\ts_mov_b64 exec, -1\n\ts_waitcnt lgkmcnt(0)" : "=&v"(r) : "v"(a), "v"(one) : "memory");
    return rfl(r);
}
DEV void lds_inc(u32 a) {
    u32 one = 1;
    asm volatile("s_mov_b64 exec, 1\n\tds_add_u32 %0, %1\n\ts_mov_b64 exec, -1" ::"v"(a), "v"(one) : "memory");
}
DEV void lds_wait_ge(u32 have, u32 need, u32 vaddr) {
    u32 vt, cn;
    asm volatile(
        "v_cmp_gt_u32 vcc, %3, %2\n\t"
        "s_cbranch_vccnz Lslow%=\n"
        "Ldone%=:\n\t"
        ".subsection 1\n"
        "Lslow%=:\n\t"
        "s_mov_b32 %1, 0x40000\n"
        "Lloop%=:\n\t"
        "ds_read_b32 %0, %4\n\t"
        "s_sub_u32 %1, %1, 1\n\t"
        "s_cbranch_scc1 Ldone%=\n\t"
        "s_waitcnt lgkmcnt(0)\n\t"
        "v_cmp_gt_u32 vcc, %3, %0\n\t"
        "s_cbranch_vccnz Lloop%=\n\t"
        "s_branch Ldone%=\n\t"
        ".subsection 0\n"
        : "=&v"(vt), "=&s"(cn) : "v"(have), "s"(need), "v"(vaddr) : "memory", "scc", "vcc");
}
DEV void lds_spin(u32 vaddr, u32 need) {
    u32 vt, cn;
    asm volatile(
        "s_mov_b32 %1, 0x40000\n"
        "Lsp%=:\n\t"
        "ds_read_b32 %0, %2\n\t"
        "s_waitcnt lgkmcnt(0)\n\t"
        "v_cmp_gt_u32 vcc, %3, %0\n\t"
        "s_cbranch_vccz Lse%=\n\t"
        "s_sleep 1\n\t"
        "s_sub_u32 %1, %1, 1\n\t"
        "s_cbranch_scc0 Lsp%=\n"
        "Lse%=:"
        : "=&v"(vt), "=&s"(cn) : "v"(vaddr), "s"(need) : "memory", "scc", "vcc");
}
DEV void g_poll1(rsrc_t rs, u32 voff, u32 tag) {
    u32 vt, cn;
    asm volatile(
        "s_mov_b32 %1, 0x8000\n"
        "Lgp%=:\n\t"
        "buffer_load_dword %0, %2, %3, 0 offen sc1\n\t"
        "s_waitcnt vmcnt(0)\n\t"
        "v_cmp_ne_u32 vcc, %4, %0\n\t"
        "s_cbranch_vccz Lge%=\n\t"
        "s_mov_b64 exec, vcc\n\t"
        "s_sleep 4\n\t"
        "s_sub_u32 %1, %1, 1\n\t"
        "s_cbranch_scc0 Lgp%=\n"
        "Lge%=:\n\t"
        "s_mov_b64 exec, -1"
        : "=&v"(vt), "=&s"(cn) : "v"(voff), "s"(rs), "s"(tag) : "memory", "scc", "vcc");
}
DEV void g_poll2(rsrc_t rs, u32 o0, u32 o1, u32 tag) {
    u32 v0, v1, cn;
    asm volatile(
        "s_mov_b32 %2, 0x4000\n"
        "Lgp%=:\n\t"
        "buffer_load_dword %0, %3, %5, 0 offen sc1\n\t"
        "buffer_load_dword %1, %4, %5, 0 offen sc1\n\t"
        "s_waitcnt vmcnt(0)\n\t"
        "v_xor_b32 %0, %6, %0\n\t"
        "v_xor_b32 %1, %6, %1\n\t"
        "v_or_b32 %0, %0, %1\n\t"
        "v_cmp_ne_u32 vcc, 0, %0\n\t"
        "s_cbranch_vccz Lge%=\n\t"
        "s_mov_b64 exec, vcc\n\t"
        "s_sleep 8\n\t"
        "s_sub_u32 %2, %2, 1\n\t"
        "s_cbranch_scc0 Lgp%=\n"
        "Lge%=:\n\t"
        "s_mov_b64 exec, -1"
        : "=&v"(v0), "=&v"(v1), "=&s"(cn) : "v"(o0), "v"(o1), "s"(rs), "s"(tag) : "memory", "scc", "vcc");
}
DEV void g_poll4(rsrc_t rs, u32 o0, u32 o1, u32 o2, u32 o3, u32 tag) {
    u32 v0, v1, v2, v3, cn;
    asm volatile(
        "s_mov_b32 %4, 0x4000\n"
        "Lgp%=:\n\t"
        "buffer_load_dword %0, %5, %9, 0 offen sc1\n\t"
        "buffer_load_dword %1, %6, %9, 0 offen sc1\n\t"
        "buffer_load_dword %2, %7, %9, 0 offen sc1\n\t"
        "buffer_load_dword %3, %8, %9, 0 offen sc1\n\t"
        "s_waitcnt vmcnt(0)\n\t"
        "v_xor_b32 %0, %10, %0\n\t"
        "v_xor_b32 %1, %10, %1\n\t"
        "v_xor_b32 %2, %10, %2\n\t"
        "v_xor_b32 %3, %10, %3\n\t"
        "v_or3_b32 %0, %0, %1, %2\n\t"
        "v_or_b32 %0, %0, %3\n\t"
        "v_cmp_ne_u32 vcc, 0, %0\n\t"
        "s_cbranch_vccz Lge%=\n\t"
        "s_mov_b64 exec, vcc\n\t"
        "s_sleep 12\n\t"
        "s_sub_u32 %4, %4, 1\n\t"
        "s_cbranch_scc0 Lgp%=\n"
        "Lge%=:\n\t"
        "s_mov_b64 exec, -1"
        : "=&v"(v0), "=&v"(v1), "=&v"(v2), "=&v"(v3), "=&s"(cn) : "v"(o0), "v"(o1), "v"(o2), "v"(o3), "s"(rs), "s"(tag) : "memory", "scc", "vcc");
}

#define TS2(i)
struct TSt {};
#define TS(i)
struct Args { const u16 *x, *k, *v, *sk, *hk, *dv, *wg, *wu, *wd; u16* out; char* ws; };

struct KvRegs { u32x2 kk[11], vv[8]; };
DEV u32x2 ld64n(rsrc_t rs, u32 voff, u32 soff) { return __builtin_amdgcn_raw_buffer_load_b64(rs, voff, soff, 2); }
DEV void kv_issue(const Args& A, KvRegs& R, int r32, int cg, int lane) {
    const int r0 = r32 * 32 + (lane >> 4) * 8;
    rsrc_t rk = mkrs(A.k, S * D * 2), rv = mkrs(A.v, S * D * 2);
    const u32 vo = r32 < 64 ? r0 * (D * 2) + cg * 128 + (lane & 15) * 8 : OOB, vp = (r0 && r32 < 64) ? vo - 3 * D * 2 : OOB;
#pragma unroll
    for (int i = 0; i < 11; ++i) R.kk[i] = i < 3 ? ld64n(rk, vp, i * D * 2) : ld64n(rk, vo, (i - 3) * D * 2);
#pragma unroll
    for (int i = 0; i < 8; ++i) R.vv[i] = ld64n(rv, vo, i * D * 2);
}
DEV void kv_finish(const KvRegs& R, const u32x4 (&w)[2], rsrc_t rw, int r32, int cg, int lane) {
    const u32 so = r32 < 64 ? (lane >> 2) * 4 * 4096 + (2016 - r32 * 32) * 2 + (lane & 3) * 16 : OOB, pl = ((3 - (lane & 3)) * 16 + (lane >> 2)) * 4;
    u32x4 p[4];
#pragma unroll
    for (int c = 0; c < 4; ++c) {
        const u32 wa = w[c >> 1][(c & 1) * 2], wb = w[c >> 1][(c & 1) * 2 + 1];
        const float w0 = blo(wa), w1 = bhi(wa), w2 = blo(wb), w3 = bhi(wb);
        float kf[11], o[8];
#pragma unroll
        for (int i = 0; i < 11; ++i) kf[i] = (c & 1) ? bhi(R.kk[i][c >> 1]) : blo(R.kk[i][c >> 1]);
#pragma unroll
        for (int i = 0; i < 8; ++i) {
            o[i] = __builtin_fmaf(kf[i + 3], w0, __builtin_fmaf(kf[i + 2], w1, __builtin_fmaf(kf[i + 1], w2, kf[i] * w3))) * ((c & 1) ? bhi(R.vv[i][c >> 1]) : blo(R.vv[i][c >> 1]));
            asm("" : "+v"(o[i]));
        }
#pragma unroll
        for (int j = 0; j < 4; ++j) p[c][j] = __builtin_amdgcn_ds_bpermute(pl, pk2(o[7 - 2 * j], o[6 - 2 * j]));
    }
#pragma unroll
    for (int c = 0; c < 4; ++c) st128(p[c], so, rw, WS_KV + (cg * 64 + c) * 4096);
}
DEV void wd_issue(const Args& A, int pu, u32 lb, int lane) {
    const int s = pu / 5, l2 = pu - s * 5;
    rsrc_t rd = mkrs(A.wd, DF * D * 2);
    const u32 vo = (32 * s + (lane >> 3)) * (D * 2) + l2 * 256 + (lane & 7) * 16;
#pragma unroll
    for (int i = 0; i < 8; ++i) dma16n(lb + i * 1024, vo, rd, (i & 3) * 8 * D * 2 + (i >> 2) * 128);
}
DEV void wd_finish(rsrc_t rw, int pu, u32 lb, int lane) {
    const int s = pu / 5, l2 = pu - s * 5;
    const u32 ta = lb + (lane >> 2) * 128 + (lane & 3) * 8;
#pragma unroll
    for (int f = 0; f < 8; ++f) {
        u32x2 a = ldstru(ta + (f & 3) * 32 + (f >> 2) * 4096), b = ldstru(ta + (f & 3) * 32 + (f >> 2) * 4096 + 2048);
        st128(u32x4{a[0], a[1], b[0], b[1]}, lane * 16, rw, WS_WD + ((8 * l2 + f) * 54 + s) * 1024);
    }
}
template <int CTRL> DEV float dppadd(float v) { return v + __uint_as_float(__builtin_amdgcn_update_dpp(0u, __float_as_uint(v), CTRL, 0xf, 0xf, true)); }
DEV void row_stats(const u32x4 (&v)[12], rsrc_t rw, int un, u32 tag, int lane) {
    const bf16x8 one = asbf(u32x4{0x3F803F80u, 0x3F803F80u, 0x3F803F80u, 0x3F803F80u});
    float s1[2], s2[2];
#pragma unroll
    for (int a = 0; a < 2; ++a) {
        f32x4 q = {0.f, 0.f, 0.f, 0.f}, s = q;
#pragma unroll
        for (int i = 0; i < 5; ++i) { q = MFMA16(asbf(v[a * 5 + i]), asbf(v[a * 5 + i]), q); s = MFMA16(one, asbf(v[a * 5 + i]), s); }
        const float t = (lane & 2) ? ((lane & 1) ? q[3] : q[2]) : ((lane & 1) ? q[1] : q[0]);
        s2[a] = dppadd<0x4E>(dppadd<0xB1>(t)); s1[a] = dppadd<0x4E>(dppadd<0xB1>(s[0]));
    }
    const float mean = ((lane & 1) ? s1[1] : s1[0]) * (1.f / D);
    const float var = __builtin_fmaxf(__builtin_fmaf(-mean, mean, ((lane & 1) ? s2[1] : s2[0]) * (1.f / D)), 0.f);
    const float den = __builtin_sqrtf(bfr(bfr(var) + 1e-5f));
    st64(u32x2{pk2(mean, den), tag}, (lane & 14) == (lane >> 4) * 4 ? WS_ST + (un * 8 + (lane & 1) * 4 + (lane >> 4)) * 8 : OOB, rw, 0);
}
DEV void st_gather(rsrc_t rw, int g0, u32 tag, int lane) {
    u32x4 sv[2];
    const u32 o0 = WS_ST + min(g0 + lane * 2, S - 2) * 8, o1 = WS_ST + min(g0 + 128 + min(lane, 49) * 2, S - 2) * 8;
#pragma unroll 1
    for (int it = 0; it < 0x4000; ++it) {
        asm volatile("" ::: "memory");
        sv[0] = ld128c(rw, o0, 0); sv[1] = ld128c(rw, o1, 0);
        const u32 bad = (sv[0][1] ^ tag) | (sv[0][3] ^ tag) | (sv[1][1] ^ tag) | (sv[1][3] ^ tag);
        if (__builtin_amdgcn_ballot_w64(bad != 0u) == 0) break;
        __builtin_amdgcn_s_sleep(12);
    }
    asm volatile("" ::: "memory");
    const float K1 = -2.f * 1.4426950408889634f * 0.7978845608028654f;
#pragma unroll
    for (int i = 0; i < 2; ++i)
#pragma unroll
        for (int j = 0; j < 2; ++j) {
            const float m = blo(sv[i][2 * j]), den = bhi(sv[i][2 * j]);
            const float r = __builtin_amdgcn_rcpf(den);
            sts128f(EPI + (i * 128 + lane * 2 + j) * 16, f32x4{-m, K1 * r, K1 * 0.044715f * r * r * r, den * den});
        }
}

DEV int wrapn(int v, int n) { return v >= n ? v - n : v; }
DEV int g2_step(int off, int w, int i) { return wrapn(off + (i < 6 ? 8 * i + w : i == 6 ? 52 + (w & 1) : 48 + w), 54); }
DEV u32 a_area(int w) { return w < 2 ? w * 20480 : 112896 + (w - 2) * 20480; }

DEV void g1_compute(int c, int lane, rsrc_t rw, int rt, int grp, TSt& T) {
    const int wi = c >> 1, wj = c & 1;
    const int tok = lane & 15, kq = lane >> 4;
    u32 xa[2], xe[2], wa[2];
    int xm = tok < 8 ? 1 : 0;
    {
        int q = (lane >> 2) & 3, p = lane & 3;
        int f = ((q >> 1) & 1) | ((kq & 1) << 1);
#pragma unroll
        for (int e = 0; e < 2; ++e) {
            wa[e] = WO + 2 * WSZ + (8 * kq + q) * 128 + (((2 * wj + (wi ^ e)) ^ f) << 5) + p * 8;
            u32 t = XS + tok * 128 + (((4 * e + kq) ^ (tok >> 1)) << 4);
            xa[e] = t + wi * 7 * 2048; xe[e] = tok < 8 ? t + 14 * 2048 : ONES;
        }
    }
    f32x4 acc[8][4];
#pragma unroll
    for (int b = 0; b < 8; ++b)
#pragma unroll
        for (int u = 0; u < 4; ++u) acc[b][u] = f32x4{0.f, 0.f, 0.f, 0.f};
    bf16x8 xf[2][8], wf[2][4];
    auto ldf = [&](int ks) {
#pragma unroll
        for (int u = 0; u < 4; ++u) { u32 a = wa[u & 1] + ks * 4096 + (u >> 1) * 8192; wf[ks][u] = cat(ldstr(a), ldstr(a + 512)); }
#pragma unroll
        for (int b = 0; b < 7; ++b) xf[ks][b] = lds128(xa[ks] + b * 2048);
        xf[ks][7] = lds128(xe[ks]);
    };
    auto mm7 = [&](int ks) {
        acc[7][0] = MFMA16(wf[ks][0], xf[ks][7], acc[7][0]);
        acc[7][2] = MFMA16(wf[ks][2], xf[ks][7], acc[7][2]);
    };
    auto mm = [&](int ks, int b0, int b1) {
#pragma unroll
        for (int b = b0; b < b1; ++b)
#pragma unroll
            for (int u = 0; u < 4; ++u) acc[b][u] = MFMA16(wf[ks][u], xf[ks][b], acc[b][u]);
    };
    auto pin = [&]() {
#pragma unroll
        for (int b = 0; b < 7; ++b) asm volatile("" : "+v"(acc[b][0]), "+v"(acc[b][1]), "+v"(acc[b][2]), "+v"(acc[b][3]));
        asm volatile("" : "+v"(acc[7][0]), "+v"(acc[7][2]));
        SB();
    };
    lds_spin(L_CNT0, 4);
    TS(1);
    u32 fa = L_FULL + (lane & 1) * 4, fw = L_FREE + c * 4, cnt = 1;
    asm volatile("" : "+v"(fa), "+v"(fw), "+v"(cnt), "+v"(xm));
    int xs = 1, wsl = 2;
    u32 have;
    auto halfA = [&]() {
        ldf(1);
        ldsflagw(fw, cnt);
        have = ldsflag1(fa);
        const int dx = xs == 2 ? -2 * (int)XS : (int)XS, dw = wsl == 3 ? -3 * (int)WSZ : (int)WSZ;
        xs = xs == 2 ? 0 : xs + 1; wsl = (wsl + 1) & 3;
        mm(0, 0, 7); mm7(0);
#pragma unroll
        for (int e = 0; e < 2; ++e) { xa[e] += dx; xe[e] += __mul24(xm, dx); wa[e] += dw; }
        cnt += 1;
#pragma unroll
        for (int i = 0; i < 16; ++i) { __builtin_amdgcn_sched_group_barrier(0x008, 1, 0); __builtin_amdgcn_sched_group_barrier(0x100, 1, 0); }
        __builtin_amdgcn_sched_group_barrier(0x008, 1, 0); __builtin_amdgcn_sched_group_barrier(0x200, 1, 0);
        __builtin_amdgcn_sched_group_barrier(0x008, 1, 0); __builtin_amdgcn_sched_group_barrier(0x100, 1, 0);
#pragma unroll
        for (int i = 0; i < 8; ++i) { __builtin_amdgcn_sched_group_barrier(0x008, 1, 0); __builtin_amdgcn_sched_group_barrier(0x002, 1, 0); }
        __builtin_amdgcn_sched_group_barrier(0x008, 4, 0);
        asm volatile("" : "+v"(xa[0]), "+v"(xa[1]), "+v"(xe[0]), "+v"(xe[1]), "+v"(wa[0]), "+v"(wa[1]), "+v"(cnt), "+v"(have));
        pin();
    };
    ldf(0);
    pin();
    halfA();
#pragma unroll 1
    for (int st = 0; st < 9; ++st) {
        lds_wait_ge(have, st + 1, fa);
        ldf(0);
        mm(1, 0, 7); mm7(1);
#pragma unroll
        for (int i = 0; i < 16; ++i) { __builtin_amdgcn_sched_group_barrier(0x008, 1, 1); __builtin_amdgcn_sched_group_barrier(0x100, 1, 1); }
        __builtin_amdgcn_sched_group_barrier(0x008, 14, 1);
        pin();
        halfA();
    }
    mm7(1); mm(1, 0, 2);
    SB();
    if (tok == 8) {
        sts128f(CSX + (2 * wj + wi) * 64 + kq * 16, acc[7][0]);
        sts128f(CSX + 256 + (2 * wj + wi) * 64 + kq * 16, acc[7][2]);
    }
    lds_inc(L_CS + wj * 4);
    SB();
    mm(1, 2, 7);
    pin();
    TS(2);
    lds_spin(lane & 1 ? L_EPI : L_CS + wj * 4, 2);
    TS(3);
    f32x4 cs[4];
#pragma unroll
    for (int u = 0; u < 4; ++u) cs[u] = lds128f(CSX + (u >> 1) * 256 + (2 * wj + (wi ^ (u & 1))) * 64 + kq * 16);
    auto act2 = [&](const f32x4& e, const f32x4& g, const f32x4& u, const f32x4& cg, const f32x4& cu, int i) {
        const f32x2 e0 = {e[0], e[0]}, e1 = {e[1], e[1]}, e2 = {e[2], e[2]}, e3 = {e[3], e[3]};
        const f32x2 a = __builtin_elementwise_fma(e0, f32x2{cg[i], cg[i + 1]}, f32x2{g[i], g[i + 1]});
        const f32x2 b = __builtin_elementwise_fma(e0, f32x2{cu[i], cu[i + 1]}, f32x2{u[i], u[i + 1]});
        const f32x2 z = a * __builtin_elementwise_fma(a * a, e2, e1);
        const f32x2 ex = {__builtin_amdgcn_exp2f(z[0]), __builtin_amdgcn_exp2f(z[1])};
        const f32x2 d = __builtin_elementwise_fma(ex, e3, e3);
        const f32x2 r = (a * b) * f32x2{__builtin_amdgcn_rcpf(d[0]), __builtin_amdgcn_rcpf(d[1])};
        return pk2(r[0], r[1]);
    };
    const int gl = ((rt * RT) & 15) + tok;
    const u32 hv = (gl >> 4) * (54 * 1024) + (kq * 16 + (gl & 15)) * 16;
    const u32 hbase = WS_H + ((((rt * RT) >> 4) + wi * 7) * 54 + grp * 2 + wj) * 1024;
#pragma unroll
    for (int b = 0; b < 7; ++b) {
        f32x4 e = lds128f(EPI + ((wi * 7 + b) * 16 + tok) * 16);
        u32 o[4];
#pragma unroll
        for (int h = 0; h < 2; ++h) { o[2 * h] = act2(e, acc[b][h], acc[b][2 + h], cs[h], cs[2 + h], 0); o[2 * h + 1] = act2(e, acc[b][h], acc[b][2 + h], cs[h], cs[2 + h], 2); }
        u32x4 ov = wi ? u32x4{o[2], o[3], o[0], o[1]} : u32x4{o[0], o[1], o[2], o[3]};
        st128(ov, hv, rw, hbase + b * 54 * 1024);
    }
    if (rt < 8) {
        f32x4 e = lds128f(EPI + (14 * 16 + tok) * 16);
        u32x2 ov = {act2(e, acc[7][0], acc[7][2], cs[0], cs[2], 0), act2(e, acc[7][0], acc[7][2], cs[0], cs[2], 2)};
        st64(ov, tok < 4 ? hv : OOB, rw, hbase + (14 - wi * 7) * 54 * 1024 + wi * 8);
    }
}
struct G1Src { rsrc_t rx, rg, ru; u32 xv0, xv1, xl, wv0, wv1; int ro; };
DEV void g1_loader(int e, const G1Src& g, int lane, rsrc_t rw) {
    const u32 fr = L_FREE + (lane & 3) * 4, fu = L_FULL + e * 4;
    const u32 xv = e ? g.xv1 : g.xv0, wv = e ? g.wv1 : g.wv0;
    auto ld_w = [&](int s) {
        const u32 so = WO + ((s + 2) & 3) * WSZ + e * 1024, ks = wrapn(s + g.ro, 10) * (64 * DF * 2) + e * 8 * DF * 2;
#pragma unroll
        for (int p = 0; p < 16; p += 2) dma16(so + p * 1024, wv, (p >> 3) ? g.ru : g.rg, ks + (p & 7) * 8 * DF * 2);
    };
    lds_spin(L_ISS0, 4);
    ld_w(1);
    int xs = 2;
#pragma unroll 1
    for (int s = 1; s < 10; ++s) {
        if (s > 2 && __builtin_amdgcn_ballot_w64(ldsflag1(fr) < (u32)(s - 2)) != 0) {
            WAITV(8);
            ldsflagw(fu, s - 1);
            lds_spin(fr, s - 2);
        }
        TS2(s);
        const u32 so = xs * XS + e * 1024, ks = wrapn(s + g.ro, 10) * 128 + e * 8 * D * 2;
        xs = xs == 2 ? 0 : xs + 1;
#pragma unroll
        for (int p = 0; p < 28; p += 2) {
            dma16(so + p * 1024, xv, g.rx, ks + p * 8 * D * 2);
            if (p == 14) { WAITV(16); ldsflagw(fu, s - 1); }
        }
        if (!e) dma16(so + 28 * 1024, g.xl, g.rx, ks + 28 * 8 * D * 2);
        if (s < 9) ld_w(s + 1);
    }
    lds_inc(L_LDONE);
    WAITV(0);
    ldsflagw(fu, 9);
    TS2(10);
}

DEV void conv_build(const u32x4 (&v)[4], u32 ib, int lane) {
    const u32 ua = ib + (lane >> 4) * 320 + (lane & 15) * 16;
#pragma unroll
    for (int i = 0; i < 4; ++i) {
        u32 sel = lane == 0 ? (i < 3 ? v[i < 3 ? i + 1 : 0][0] : 0u) : v[i][0];
        u32 nx = __builtin_amdgcn_ds_bpermute(((lane + 1) & 63) * 4, sel);
        sts128(ua + IMG_U0 + i * 1280, v[i]);
        sts128(ua + IMG_U1 + i * 1280, u32x4{__builtin_amdgcn_alignbit(v[i][1], v[i][0], 16), __builtin_amdgcn_alignbit(v[i][2], v[i][1], 16),
                                             __builtin_amdgcn_alignbit(v[i][3], v[i][2], 16), __builtin_amdgcn_alignbit(nx, v[i][3], 16)});
    }
    if (lane < 36) sts128(lane < 32 ? ib + (lane < 16 ? IMG_U0 : IMG_U1) + 5120 + (lane & 15) * 16 : ib + (lane - 32) * 16, u32x4{0u, 0u, 0u, 0u});
}
DEV void conv_task(rsrc_t rw, int ch, int g, float dv, u32 ib, u32 yst, int lane, TSt& T) {
    const int a = lane & 15, q = lane >> 4;
    u32 ha = ib + 4 + 4 * a + 16 * q;
    const u32 ub = ib + ((a & 1) ? IMG_U0 : IMG_U1) + 16 * q;
    u32 ua = ub + (15 - 8 * g - (a >> 1)) * 320, un;
    const u32 uz = ub + 5120;
    f32x4 acc[4];
#pragma unroll
    for (int j = 0; j < 8; ++j) {
        const int e = 2046 - 1024 * g - (32 * (j >> 1) + 2 * a + 128 * (2 * q + (j & 1)));
        const u32 uu = lds32(ib + IMG_U0 + (e >> 7) * 320 + (e & 127) * 2);
        acc[j >> 1][2 * (j & 1)] = bhi(uu) * dv; acc[j >> 1][2 * (j & 1) + 1] = blo(uu) * dv;
    }
    u32 nit = 4 * g + 3;
    asm volatile(
        "ds_read_b128 v[240:243], %4 offset:0\n\t"
        "ds_read2_b32 v[208:209], %6 offset0:0 offset1:1\n\t"
        "ds_read2_b32 v[210:211], %6 offset0:2 offset1:3\n\t"
        "ds_read_b128 v[244:247], %4 offset:64\n\t"
        "ds_read2_b32 v[212:213], %6 offset0:16 offset1:17\n\t"
        "ds_read2_b32 v[214:215], %6 offset0:18 offset1:19\n\t"
        "ds_read_b128 v[248:251], %4 offset:128\n\t"
        "ds_read2_b32 v[216:217], %6 offset0:32 offset1:33\n\t"
        "ds_read2_b32 v[218:219], %6 offset0:34 offset1:35\n\t"
        "v_mov_b32 v228, 0\n\t"
        "v_mov_b32 v229, 0\n\t"
        "v_mov_b32 v230, 0\n\t"
        "v_mov_b32 v231, 0\n\t"
        "v_mov_b32 v232, 0\n\t"
        "v_mov_b32 v233, 0\n\t"
        "v_mov_b32 v234, 0\n\t"
        "v_mov_b32 v235, 0\n\t"
        "v_mov_b32 v236, 0\n\t"
        "v_mov_b32 v237, 0\n\t"
        "v_mov_b32 v238, 0\n\t"
        "v_mov_b32 v239, 0\n"
        "Lcv%=:\n\t"
        "s_waitcnt lgkmcnt(6)\n\t"
        "v_mfma_f32_16x16x32_bf16 %0, v[240:243], v[228:231], %0\n\t"
        "ds_read_b128 v[252:255], %4 offset:192\n\t"
        "v_mfma_f32_16x16x32_bf16 %1, v[240:243], v[232:235], %1\n\t"
        "ds_read2_b32 v[220:221], %6 offset0:48 offset1:49\n\t"
        "v_mfma_f32_16x16x32_bf16 %2, v[240:243], v[236:239], %2\n\t"
        "ds_read2_b32 v[222:223], %6 offset0:50 offset1:51\n\t"
        "v_add_u32 %5, 0x140, %4\n\t"
        "v_mfma_f32_16x16x32_bf16 %3, v[240:243], v[208:211], %3\n\t"
        "v_min_u32 %5, %5, %8\n\t"
        "s_waitcnt lgkmcnt(6)\n\t"
        "v_mfma_f32_16x16x32_bf16 %0, v[244:247], v[232:235], %0\n\t"
        "ds_read_b128 v[240:243], %5 offset:0\n\t"
        "v_mfma_f32_16x16x32_bf16 %1, v[244:247], v[236:239], %1\n\t"
        "ds_read2_b32 v[224:225], %6 offset0:64 offset1:65\n\t"
        "v_mfma_f32_16x16x32_bf16 %2, v[244:247], v[208:211], %2\n\t"
        "ds_read2_b32 v[226:227], %6 offset0:66 offset1:67\n\t"
        "v_mfma_f32_16x16x32_bf16 %3, v[244:247], v[212:215], %3\n\t"
        "s_waitcnt lgkmcnt(6)\n\t"
        "v_mfma_f32_16x16x32_bf16 %0, v[248:251], v[236:239], %0\n\t"
        "ds_read_b128 v[244:247], %5 offset:64\n\t"
        "v_mfma_f32_16x16x32_bf16 %1, v[248:251], v[208:211], %1\n\t"
        "ds_read2_b32 v[228:229], %6 offset0:80 offset1:81\n\t"
        "v_mfma_f32_16x16x32_bf16 %2, v[248:251], v[212:215], %2\n\t"
        "ds_read2_b32 v[230:231], %6 offset0:82 offset1:83\n\t"
        "v_add_u32 %4, 0x140, %5\n\t"
        "v_mfma_f32_16x16x32_bf16 %3, v[248:251], v[216:219], %3\n\t"
        "v_min_u32 %4, %4, %8\n\t"
        "s_waitcnt lgkmcnt(6)\n\t"
        "v_mfma_f32_16x16x32_bf16 %0, v[252:255], v[208:211], %0\n\t"
        "ds_read_b128 v[248:251], %5 offset:128\n\t"
        "v_mfma_f32_16x16x32_bf16 %1, v[252:255], v[212:215], %1\n\t"
        "ds_read2_b32 v[232:233], %6 offset0:96 offset1:97\n\t"
        "v_mfma_f32_16x16x32_bf16 %2, v[252:255], v[216:219], %2\n\t"
        "ds_read2_b32 v[234:235], %6 offset0:98 offset1:99\n\t"
        "v_mfma_f32_16x16x32_bf16 %3, v[252:255], v[220:223], %3\n\t"
        "s_waitcnt lgkmcnt(6)\n\t"
        "v_mfma_f32_16x16x32_bf16 %0, v[240:243], v[212:215], %0\n\t"
        "ds_read_b128 v[252:255], %5 offset:192\n\t"
        "v_mfma_f32_16x16x32_bf16 %1, v[240:243], v[216:219], %1\n\t"
        "ds_read2_b32 v[236:237], %6 offset0:112 offset1:113\n\t"
        "v_mfma_f32_16x16x32_bf16 %2, v[240:243], v[220:223], %2\n\t"
        "ds_read2_b32 v[238:239], %6 offset0:114 offset1:115\n\t"
        "v_mfma_f32_16x16x32_bf16 %3, v[240:243], v[224:227], %3\n\t"
        "s_waitcnt lgkmcnt(6)\n\t"
        "v_mfma_f32_16x16x32_bf16 %0, v[244:247], v[216:219], %0\n\t"
        "ds_read_b128 v[240:243], %4 offset:0\n\t"
        "v_mfma_f32_16x16x32_bf16 %1, v[244:247], v[220:223], %1\n\t"
        "ds_read2_b32 v[208:209], %6 offset0:128 offset1:129\n\t"
        "v_mfma_f32_16x16x32_bf16 %2, v[244:247], v[224:227], %2\n\t"
        "ds_read2_b32 v[210:211], %6 offset0:130 offset1:131\n\t"
        "v_mfma_f32_16x16x32_bf16 %3, v[244:247], v[228:231], %3\n\t"
        "s_waitcnt lgkmcnt(6)\n\t"
        "v_mfma_f32_16x16x32_bf16 %0, v[248:251], v[220:223], %0\n\t"
        "ds_read_b128 v[244:247], %4 offset:64\n\t"
        "v_mfma_f32_16x16x32_bf16 %1, v[248:251], v[224:227], %1\n\t"
        "ds_read2_b32 v[212:213], %6 offset0:144 offset1:145\n\t"
        "v_mfma_f32_16x16x32_bf16 %2, v[248:251], v[228:231], %2\n\t"
        "ds_read2_b32 v[214:215], %6 offset0:146 offset1:147\n\t"
        "v_mfma_f32_16x16x32_bf16 %3, v[248:251], v[232:235], %3\n\t"
        "s_sub_u32 %7, %7, 1\n\t"
        "s_waitcnt lgkmcnt(6)\n\t"
        "v_mfma_f32_16x16x32_bf16 %0, v[252:255], v[224:227], %0\n\t"
        "ds_read_b128 v[248:251], %4 offset:128\n\t"
        "v_mfma_f32_16x16x32_bf16 %1, v[252:255], v[228:231], %1\n\t"
        "ds_read2_b32 v[216:217], %6 offset0:160 offset1:161\n\t"
        "v_mfma_f32_16x16x32_bf16 %2, v[252:255], v[232:235], %2\n\t"
        "ds_read2_b32 v[218:219], %6 offset0:162 offset1:163\n\t"
        "v_mfma_f32_16x16x32_bf16 %3, v[252:255], v[236:239], %3\n\t"
        "v_add_u32 %6, 0x200, %6\n\t"
        "s_cbranch_scc0 Lcv%=\n\t"
        "s_waitcnt lgkmcnt(0)\n\t"
        "s_nop 7\n\t"
        "s_nop 7\n\t"
        : "+v"(acc[0]), "+v"(acc[1]), "+v"(acc[2]), "+v"(acc[3]), "+v"(ua), "=&v"(un), "+v"(ha), "+s"(nit)
        : "v"(uz)
        : "memory", "scc", "v208", "v209", "v210", "v211", "v212", "v213", "v214", "v215", "v216", "v217", "v218", "v219", "v220", "v221", "v222", "v223", "v224", "v225", "v226", "v227", "v228", "v229", "v230", "v231", "v232", "v233", "v234", "v235", "v236", "v237", "v238", "v239", "v240", "v241", "v242", "v243", "v244", "v245", "v246", "v247", "v248", "v249", "v250", "v251", "v252", "v253", "v254", "v255");
    TS(10);
#pragma unroll
    for (int j = 0; j < 8; ++j) sts32(yst + (32 * (j >> 1) + 2 * a + 128 * (2 * q + (j & 1))) * 2, pk2(acc[j >> 1][2 * (j & 1)], acc[j >> 1][2 * (j & 1) + 1]));
    asm volatile("" ::: "memory");
    const u32x4 y0 = lds128u(yst + lane * 16), y1 = lds128u(yst + 1024 + lane * 16);
    st128(y0, lane * 16, rw, WS_Y + ch * 4096 + g * 2048);
    st128(y1, lane * 16, rw, WS_Y + ch * 4096 + g * 2048 + 1024);
}

DEV void g2_ldw(bf16x8 (&wr)[5], rsrc_t rw, u32 vo, u32 wsrc, int ks) {
#pragma unroll
    for (int f = 0; f < 5; ++f) wr[f] = asbf(ld128c(rw, vo, wsrc + (f * 54 + ks) * 1024));
}
DEV void g2_ldw6(bf16x8 (&wr)[6][5], rsrc_t rw, u32 wsrc, int off, int wid, int lane, int j0, int j1) {
#pragma unroll
    for (int j = j0; j < j1; ++j) {
        wr[j / 5][j % 5] = asbf(ld128c(rw, lane * 16, wsrc + ((j % 5) * 54 + g2_step(off, wid, j / 5)) * 1024));
        if (j >= THR && j < 29) asm volatile("s_waitcnt vmcnt(%0)" ::"n"(THR) : "memory");
    }
}
DEV void g2_fin(const Args& A, rsrc_t rw, bf16x8 (&wr)[6][5], u32 wsrc, int w, int off, int blk, int cgp, int lane, TSt& T) {
    f32x4 acc[4][5];
#pragma unroll
    for (int t = 0; t < 4; ++t)
#pragma unroll
        for (int f = 0; f < 5; ++f) acc[t][f] = f32x4{0.f, 0.f, 0.f, 0.f};
    bf16x8 hf[8][4];
    const u32 vz = w >= 2 ? OOB : (u32)lane * 16;
    auto ldh = [&](int i) {
#pragma unroll
        for (int t = 0; t < 4; ++t) hf[i][t] = asbf(ld128c(rw, i == 6 ? vz : (u32)lane * 16, WS_H + ((blk + ((t + 1 + w) & 3)) * 54 + g2_step(off, w, i)) * 1024));
    };
    ldh(0); ldh(1);
#pragma unroll
    for (int i = 0; i < 8; ++i) {
        if (i == 7) lds_spin(L_YCNT, 4);
#pragma unroll
        for (int t = 0; t < 4; ++t) {
#pragma unroll
            for (int f = 0; f < 5; ++f) acc[t][f] = MFMA16(wr[i % 6][f], hf[i][t], acc[t][f]);
            if (i == 7 && t > 0) {
#pragma unroll
                for (int f = 0; f < 5; ++f) sts128f(RB + ((((t + w) & 3) * 3 + t - 1) * 5 + f) * 1024 + lane * 16, acc[t - 1][f]);
                SB();
            }
        }
        SB();
        if (i + 2 < 8) ldh(i + 2);
        if (i < 2) g2_ldw(wr[i], rw, i == 0 ? vz : (u32)lane * 16, wsrc, g2_step(off, w, 6 + i));
        SB();
    }
    lds_inc(L_BD);
    TS(7);
    lds_spin(L_AS, 4);
#pragma unroll
    for (int f = 0; f < 5; ++f) acc[3][f] += lds128f(a_area(w) + (15 + f) * 1024 + lane * 16);
    TS(8);
    lds_spin(L_BD, 4);
    TS(9);
    f32x4 pr[15];
#pragma unroll
    for (int j = 0; j < 15; ++j) pr[j] = lds128f(RB + (w * 15 + j) * 1024 + lane * 16);
    const u32 ta = a_area(w) + 15 * 1024;
#pragma unroll
    for (int f = 0; f < 5; ++f) {
        const f32x4 o = (acc[3][f] + pr[f]) + (pr[5 + f] + pr[10 + f]);
        *(AS3 u32x2*)(uintptr_t)(ta + (lane & 15) * 160 + (lane >> 4) * 8 + f * 32) = u32x2{pk2(o[0], o[1]), pk2(o[2], o[3])};
    }
    asm volatile("" ::: "memory");
    rsrc_t ro = mkrs(A.out, S * D * 2);
#pragma unroll
    for (int j = 0; j < 3; ++j) {
        const int e = j * 64 + lane, r = e / 10;
        __builtin_amdgcn_raw_buffer_store_b128(lds128u(ta + e * 16), ro, e < 160 ? ((blk + w) * 16 + r) * (D * 2) + cgp * 160 + (e - r * 10) * 16 : OOB, 0, OUTAUX);
    }
}
DEV void g2_pre(const Args& A, rsrc_t rw, const bf16x8 (&wr)[6][5], int w, int off, int blk, int cgp, u32 tag, u32 sv, int lane, TSt& T) {
    f32x4 acc[4][5];
#pragma unroll
    for (int t = 0; t < 4; ++t)
#pragma unroll
        for (int f = 0; f < 5; ++f) acc[t][f] = f32x4{0.f, 0.f, 0.f, 0.f};
    bf16x8 hf[6][4];
    u32x4 yv[3];
    const u32 la = a_area(w) + lane * 16;
    auto ldh = [&](int i) {
#pragma unroll
        for (int t = 0; t < 4; ++t) hf[i][t] = asbf(ld128c(rw, lane * 16, WS_H + ((blk + ((t + 1 + w) & 3)) * 54 + g2_step(off, w + 4, i)) * 1024));
    };
    ldh(0); ldh(1);
#pragma unroll
    for (int i = 0; i < 6; ++i) {
#pragma unroll
        for (int t = 0; t < 4; ++t) {
#pragma unroll
            for (int f = 0; f < 5; ++f) acc[t][f] = MFMA16(wr[i][f], hf[i][t], acc[t][f]);
            if (i == 5 && t > 0) {
#pragma unroll
                for (int f = 0; f < 5; ++f) sts128f(la + ((t - 1) * 5 + f) * 1024, acc[t - 1][f]);
                SB();
            }
        }
        SB();
        if (i == YAT) {
            if (w == 2) { g_poll1(rw, WS_F2 + cgp * 512 + (lane & 31) * 16, tag); ldsflagw(L_YOK, 1u); }
            else lds_spin(L_YOK, 1);
#pragma unroll
            for (int j = 0; j < 3; ++j) yv[j] = ld128c(rw, (j == 2 && w >= 2) ? OOB : (lane >> 3) * 4096 + (lane & 7) * 16, WS_Y + (cgp * 80 + (j * 4 + w) * 8) * 4096 + blk * 32);
        }
        if (i == 4) {
#pragma unroll
            for (int j = 0; j < 3; ++j) if (j < 2 || w < 2) sts128(YT + (j * 4 + w) * 1024 + lane * 16, yv[j]);
            lds_inc(L_YRDY);
        }
        if (i + 2 < 6) ldh(i + 2);
        SB();
    }
    lds_inc(L_AD);
    TS(7);
    {
            lds_spin(L_YRDY, 4);
            TS(9);
            const float r = __builtin_amdgcn_rcpf(bhi(sv)), mr = -blo(sv) * r;
#pragma unroll
            for (int f = 0; f < 5; ++f) {
                const u32x2 y = ldstru(YT + (16 * f + (lane >> 2)) * 128 + w * 32 + (lane & 3) * 8);
                const u32x2 xv = *(const AS3 u32x2*)(uintptr_t)(a_area(w) + 15 * 1024 + (lane & 15) * 160 + (lane >> 4) * 8 + f * 32);
                const float xx[4] = {blo(xv[0]), bhi(xv[0]), blo(xv[1]), bhi(xv[1])}, yy[4] = {blo(y[0]), bhi(y[0]), blo(y[1]), bhi(y[1])};
#pragma unroll
                for (int j = 0; j < 4; ++j) acc[3][f][j] += __builtin_fmaf(__builtin_fmaf(xx[j], r, mr), yy[j], xx[j]);
            }
        }
    lds_spin(L_AD, 4);
    TS(8);
    f32x4 pr[15];
#pragma unroll
    for (int j = 0; j < 15; ++j) pr[j] = lds128f(a_area((w + 3 - j / 5) & 3) + j * 1024 + lane * 16);
#pragma unroll
    for (int f = 0; f < 5; ++f) sts128f(la + (15 + f) * 1024, (acc[3][f] + pr[f]) + (pr[5 + f] + pr[10 + f]));
    lds_inc(L_AS);
}

#define P0A 1
#define P0B 3
#define KVAT 15
#define POLLAT 6
__global__ __launch_bounds__(512, 2) void fused_kernel(Args A) {
    const int b = blockIdx.x, lane = threadIdx.x & 63, wid = rfl(threadIdx.x >> 6);
    const int xcd = b & 7, idx = b >> 3;
    rsrc_t rw = mkrs(A.ws, WS_SIZE);
    TSt T = {};
    TS(0);
    int grp = 0, rt = 0, idle = -1, ro;
    if (xcd < 6) {
        int c = (xcd & 1) * 32 + idx;
        if (c == 63) idle = xcd >> 1;
        grp = c / 3; const int r3 = c - grp * 3; rt = (xcd >> 1) * 3 + r3;
        ro = (grp + 3 * r3 + (xcd >> 1)) % 10;
    } else {
        int gi;
        if (xcd == 6) { if (idx >= 30) idle = idx - 27; gi = idx / 5; rt = idx - gi * 5; }
        else { if (idx >= 24) idle = idx - 19; gi = idx >> 2; rt = 5 + (idx & 3); }
        grp = 21 + gi;
        ro = 2 * ((gi + rt) % 5) + (xcd & 1);
    }
    const bool busy = idle < 0;
    if (threadIdx.x >= 448 && lane < 36) ldsflagw(lane < 32 ? FLG + lane * 4 : ONES + (lane - 32) * 4, lane >= 32 ? 0x3F803F80u : (!busy && lane < 8) ? 10u : 0u);
    BARL();
    G1Src g;
    g.rx = mkrs(A.x, S * D * 2); g.rg = mkrs(A.wg, D * DF * 2); g.ru = mkrs(A.wu, D * DF * 2); g.ro = ro;
    { int row = lane >> 3;
      g.xv0 = (rt * RT + row) * (D * 2) + (((lane & 7) ^ (row >> 1)) << 4); g.xv1 = g.xv0 ^ 64; g.xl = row < 4 ? g.xv0 : OOB;
      g.wv0 = row * (DF * 2) + grp * 128 + (((lane & 7) ^ (((row >> 1) & 1) << 1)) << 4); g.wv1 = g.wv0 ^ 64; }
    if (wid < 4) {
        PRIO(3);
        if (busy) {
            if (wid & 1) { g.xv0 = g.xv1; g.wv0 = g.wv1; g.xl = OOB; }
            const u32 ks = ro * 128 + (wid & 1) * 8 * D * 2, kw = ro * (64 * DF * 2) + (wid & 1) * 8 * DF * 2, lo = (wid & 1) * 1024;
            if (wid < 2) {
#pragma unroll
                for (int p = 0; p < 22; p += 2) dma16(XS + lo + p * 1024, g.xv0, g.rx, ks + p * 8 * D * 2);
            } else {
#pragma unroll
                for (int p = 0; p < 16; p += 2) dma16(WO + 2 * WSZ + lo + p * 1024, g.wv0, (p >> 3) ? g.ru : g.rg, kw + (p & 7) * 8 * DF * 2);
#pragma unroll
                for (int p = 22; p < 28; p += 2) dma16(XS + lo + p * 1024, g.xv0, g.rx, ks + p * 8 * D * 2);
                if (wid == 2) dma16(XS + 28 * 1024, g.xl, g.rx, ks + 28 * 8 * D * 2);
            }
        }
    }
    if (busy && wid < 4) { lds_inc(L_ISS0); PRIO(1); WAITV(0); lds_inc(L_CNT0); }
    const int pr = (b >> 4) * 8 + xcd, mem = idx & 1;
    const int c0 = pr * 5 + mem * 3;
    u32x4 e[12];
    const u32 fr = L_FREE + (lane & 3) * 4;
    if (!busy || wid >= 6) {
        u32 cpf = 0;
        const int q = rt * 27 + grp;
        int kvt = 320, hf = 0, nkv = 1, wdp = -1, stu = -1;
        if (busy) { kvt = q; hf = wid - 6; if (wid == 7) wdp = q; else stu = q + q / 18; }
        else {
            if (wid < 6) { kvt = 243 + idle * 6 + wid; if (kvt == 320) wdp = 269; else nkv = 2; }
            else wdp = 243 + 2 * idle + wid - 6;
            if (wid == 6) stu = 19 * idle + 18;
        }
        const int r32 = (kvt / 10) * 2 + hf, cg = kvt % 10;
        const u32 lb = (busy || wid == 5) ? STG : (wid - 6) * 8192;
        if (wid == 7) {
            uint64_t pc; u32 len;
            asm volatile("s_getpc_b64 %0\nLcs%=:\n\ts_mov_b32 %1, Lce%=-Lcs%=\n\t.subsection 2\nLce%=:\n\t.subsection 0" : "=s"(pc), "=s"(len));
            cpf = __builtin_amdgcn_raw_buffer_load_b32(mkrs((const void*)pc, min(len, 40960u)), lane < 10 ? (idx * 10 + lane) * 128 : OOB, 0, 0);
        }
        u32 ep = 0;
        if (wid == 6) ep = __builtin_amdgcn_raw_buffer_load_b32(rw, 0, 0, 16);
        if (P0A) lds_spin(fr, P0A);
        if (wdp >= 0) wd_issue(A, wdp, lb, lane);
        if (wid == 6) {
#pragma unroll
            for (int i = 0; i < 10; ++i) e[i] = ld128(g.rx, (stu * 8 + ((lane >> 2) & 3)) * (D * 2) + (lane >> 4) * 64 + (lane & 3) * 16, (i / 5) * 4 * D * 2 + (i % 5) * 256);
        }
        SB();
        if (!busy) { if (wid >= 6) lds_inc(L_ISS0); else lds_spin(L_ISS0, 2); }
        PRIO(P0PRIO);
        if (wid == 6) {
            ep += 0x9E3779B1u;
            ldsflagw(L_TAG, ep);
            ldsflagw(L_TAGOK, 1u);
            row_stats(e, rw, stu, ep, lane);
        }
        if (wdp >= 0) {
            WAITV(0);
            wd_finish(rw, wdp, lb, lane);
            lds_spin(L_TAGOK, 1);
            const int s5 = wdp / 5;
            const u32 tg = ldsflag1(L_TAG);
            WAITV(0);
            st32(tg, lane == 0 ? WS_FW + (wdp - s5 * 5) * 1024 + s5 * 16 : OOB, rw);
        }
        asm volatile("" ::"v"(cpf));
        SB();
        TS2(1);
        if (P0B) lds_spin(fr, P0B);
        KvRegs R, R2;
        u32x4 w[2];
        { rsrc_t rs = mkrs(A.sk, D * 8); w[0] = ld128(rs, cg * 512 + (lane & 15) * 32, 0); w[1] = ld128(rs, cg * 512 + (lane & 15) * 32, 16); }
        kv_issue(A, R, r32, cg, lane);
        if (nkv == 2) kv_issue(A, R2, r32 + 1, cg, lane);
        if (wid == 7) {
            rsrc_t rh = mkrs(A.hk, D * S * 2);
#pragma unroll
            for (int i = 0; i < 12; ++i) e[i] = ld128n(rh, lane * 16, (i < 8 ? c0 + (i >> 2) : pr * 5 + 2) * 4096 + (i & 3) * 1024);
        }
        SB();
        TS2(2);
#pragma unroll 1
        for (int r = 0; r < nkv; ++r) {
            kv_finish(R, w, rw, r32 + r, cg, lane);
            R = R2;
        }
        TS2(3);
        lds_spin(L_TAGOK, 1);
        const u32 tg = ldsflag1(L_TAG);
        WAITV(0);
        st32(tg, (lane < nkv && kvt < 320) ? WS_FK + cg * 1024 + (r32 + lane) * 16 : OOB, rw);
        PRIO(0);
    }
    const int cgp = (xcd & 1) * 4 + (idx & 3), jr = idx >> 2, blk = ((xcd >> 1) * 8 + jr) * 4;
    const int off = wrapn(13 * (idx & 3) + 4 * (jr & 1) + 16 * (jr >> 1), 54);
    const u32 wsrc = WS_WD + cgp * 5 * 54 * 1024;
    const int w4 = wid & 3;
    bf16x8 wr[6][5];
    if (wid < 4) {
        if (busy) {
            g1_compute(wid, lane, rw, rt, grp, T);
            TS(4);
            WAITV(0);
            TS(5);
            if (lds_add(L_HCNT) == 3u && lane == 0) st32(ldsflag1(L_TAG), WS_F1 + (rt * 32 + grp) * 128, rw);
        }
        const u32 tag = rfl(ldsflag1(L_TAG));
        if (idle == 0 && wid == 0) {
            const u32 o = WS_ST + 4 + lane * 64;
            g_poll4(rw, o, o + 4096, o + 8192, o + 12288, tag);
            if (lane == 0) st32(tag, 0, rw);
        }
        lds_spin(L_P0ALL, 1);
        g2_ldw6(wr, rw, wsrc, off, wid, lane, 0, 30);
        SB();
        TS(10);
        if (wid == 0) {
            const int r = (blk * 16 + (lane < 32 ? 0 : 63)) / RT;
            g_poll1(rw, WS_F1 + (r * 32 + min(lane & 31, 26)) * 128, tag);
            ldsflagw(L_HRDY, 1u);
        } else lds_spin(L_HRDY, 1);
        TS(6);
        PRIO(0);
        g2_fin(A, rw, wr, wsrc, wid, off, blk, cgp, lane, T);
        TS(11);
    } else {
        if (wid < 6) { if (busy) { PRIO(2); g1_loader(wid - 4, g, lane, rw); PRIO(0); } }
        TS(1);
        u32x4 v[4];
        if (wid >= 6) {
            const u32 tg = rfl(ldsflag1(L_TAG));
            lds_spin(fr, POLLAT);
            if (wid == 7) {
                if (busy) { st_gather(rw, rt * RT, tg, lane); ldsflagw(L_EPI, 2u); }
                lds_spin(L_P0ALL, 1);
            } else {
                const u32 ow = WS_FW + min(lane, 53) * 16;
                g_poll2(rw, ow + ((cgp * 5) >> 3) * 1024, ow + ((cgp * 5 + 4) >> 3) * 1024, tg);
                ldsflagw(L_P0ALL, 1u);
            }
            lds_spin(L_LDONE, 2);
        } else lds_spin(L_P0ALL, 1);
        TS(2);
        const u32 tag = rfl(ldsflag1(L_TAG));
        const u32 dvv = __builtin_amdgcn_raw_buffer_load_b16(mkrs(A.dv, D * 2), (pr * 5 + lane) * 2, 0, 0);
        g2_ldw6(wr, rw, wsrc, off, wid, lane, 0, KVAT);
        SB();
        if (wid >= 6) {
            const u32 ok = WS_FK + lane * 16;
            g_poll2(rw, ok + ((wid == 6 ? c0 : pr * 5 + 2) >> 6) * 1024, ok + ((wid == 6 ? c0 + 1 : pr * 5 + 2) >> 6) * 1024, tag);
            if (wid == 6) {
#pragma unroll
                for (int i = 0; i < 4; ++i) e[i] = ld128c(rw, WS_KV + lane * 16, (c0 + 1) * 4096 + i * 1024);
            }
#pragma unroll
            for (int i = 0; i < 4; ++i) v[i] = ld128c(rw, WS_KV + lane * 16, (wid == 6 ? c0 : pr * 5 + 2) * 4096 + i * 1024);
        }
        SB();
        g2_ldw6(wr, rw, wsrc, off, wid, lane, KVAT, 30);
        SB();
        if (wid >= 6) {
            lds_spin(fr, 8);
            conv_build(v, IMG + (wid == 6 ? 0 : 2 * IMGSZ), lane);
            if (wid == 6) { const u32x4 e4[4] = {e[0], e[1], e[2], e[3]}; conv_build(e4, IMG + IMGSZ, lane); }
            else {
#pragma unroll
                for (int i = 0; i < 12; ++i) sts128(IMG + (i >> 2) * IMGSZ + 64 + (i & 3) * 1024 + lane * 16, e[i]);
            }
            lds_inc(L_IMG);
        }
        lds_spin(L_IMG, 2);
        TS(3);
        const u32 yst = YST + w4 * 2048;
        const int nt = wid == 7 ? 2 : 1;
#pragma unroll 1
        for (int sl = 0; sl < nt; ++sl) {
            const int im = wid == 7 ? sl : w4;
            const int ci = im == 2 ? 2 : mem * 3 + im;
            conv_task(rw, pr * 5 + ci, (wid == 7 || (mem && w4 == 2)) ? 0 : 1, blo(__builtin_amdgcn_readlane(dvv, ci)), IMG + im * IMGSZ, yst, lane, T);
        }
        TS(4);
        lds_spin(fr, 10);
        WAITV(0);
        TS(5);
        if (lds_add(L_YCNT) == 3u && lane == 0) st32(tag, WS_F2 + (pr * 2 + mem) * 16, rw);
        {
            rsrc_t rx = mkrs(A.x, S * D * 2);
#pragma unroll
            for (int j = 0; j < 3; ++j) {
                const int el = j * 64 + lane, r = el / 10;
                dma16(a_area(w4) + (15 + j) * 1024, el < 160 ? ((blk + w4) * 16 + r) * (D * 2) + cgp * 160 + (el - r * 10) * 16 : OOB, rx, 0);
            }
        }
        lds_spin(L_HRDY, 1);
        TS(6);
        PRIO(1);
        const u32 sv = __builtin_amdgcn_raw_buffer_load_b32(rw, WS_ST + ((blk + w4) * 16 + (lane & 15)) * 8, 0, 16);
        g2_pre(A, rw, wr, w4, off, blk, cgp, tag, sv, lane, T);
        TS(11);
    }
}

extern "C" void kernel_launch(void* const* d_in, const int* in_sizes, int n_in, void* d_out, int out_size, void* d_ws, size_t ws_size, hipStream_t stream) {
    Args A;
    A.x = (const u16*)d_in[0]; A.k = (const u16*)d_in[1]; A.v = (const u16*)d_in[2]; A.sk = (const u16*)d_in[3]; A.hk = (const u16*)d_in[4];
    A.dv = (const u16*)d_in[5]; A.wg = (const u16*)d_in[7]; A.wu = (const u16*)d_in[8]; A.wd = (const u16*)d_in[9];
    A.out = (u16*)d_out; A.ws = (char*)d_ws;
    (void)hipFuncSetAttribute((const void*)fused_kernel, hipFuncAttributeMaxDynamicSharedMemorySize, LDS_BYTES);
    fused_kernel<<<256, 512, LDS_BYTES, stream>>>(A);
}
```

```cpp
#define EARLYEP
#define OLDORD
#define NOHPF
#include <hip/hip_runtime.h>
#include <stdint.h>
typedef unsigned short u16;
typedef unsigned int u32;
typedef __attribute__((ext_vector_type(8))) __bf16 bf16x8;
typedef __attribute__((ext_vector_type(4))) __bf16 bf16x4;
typedef __attribute__((ext_vector_type(2))) __bf16 bf16x2;
typedef __attribute__((ext_vector_type(4))) float f32x4;
typedef __attribute__((ext_vector_type(2))) float f32x2;
typedef __attribute__((ext_vector_type(4))) unsigned u32x4;
typedef __attribute__((ext_vector_type(2))) unsigned u32x2;
typedef __amdgpu_buffer_rsrc_t rsrc_t;
#define AS3 __attribute__((address_space(3)))
#define DEV __device__ __forceinline__
constexpr int S = 2048, D = 640, DF = 1728, RT = 228;
#define F2S 16
constexpr u32 WS_FP = 4096, WS_F2 = F2S == 16 ? 24576 : 90112, WS_ST = 32768, WS_F1 = 49152;
constexpr u32 WS_KV = 128u << 10, WS_Y = WS_KV + S * D * 2, WS_WD = WS_Y + S * D * 2, WS_H = WS_WD + DF * D * 2, WS_SIZE = 20u << 20;
constexpr u32 XS = 29696, WO = 3 * XS, WSZ = 16384, STG = WO + 4 * WSZ, FLG = STG + 8192, ONES = FLG + 256, CSX = FLG + 512;
constexpr u32 L_FULL = FLG, L_CNT0 = FLG + 8, L_LDONE = FLG + 12, L_FREE = FLG + 16, L_P0ALL = FLG + 32, L_EPI = FLG + 36, L_IMG = FLG + 40,
              L_HCNT = FLG + 52, L_YCNT = FLG + 56, L_HRDY = FLG + 60, L_BD = FLG + 80, L_AD = FLG + 84, L_YRDY = FLG + 88, L_TAG = FLG + 92,
              L_ISS0 = FLG + 64, L_YOK = FLG + 68, L_HM = FLG + 72, L_NIL = FLG + 76, L_AF = FLG + 44, L_CD = FLG + 48, L_CS = FLG + 96, L_AS = FLG + 104, L_TAGOK = FLG + 108, L_PCNT = FLG + 112, L_KVISS = FLG + 116, L_GD = FLG + 120, L_PCNT2 = FLG + 124;
constexpr u32 EPI = STG;
constexpr u32 IMG_U0 = 4160, IMG_U1 = 4160 + 5472, IMGSZ = 4160 + 5472 + 5440;
constexpr u32 IMG = 2 * XS, YST = IMG + 3 * IMGSZ, YT = 40960, RB = 51200;
constexpr u32 LDS_BYTES = 163840;
constexpr u32 OOB = 0x7fffffffu;
#define YN 99
#define SKIP 1
#define YD 5
#define LKA 8
#define LKB 12
#define YORD 0
#define DEEP 1
#define DEEPA 0
#define GATE 5
#define CPRIO 2
#define WG0 6
#define WGS 4
#define P0S 0
#define YAT 9
#define P0PRIO 3
#define THR 6
#define OUTAUX 0

DEV u32 rfl(u32 v) { return __builtin_amdgcn_readfirstlane(v); }
DEV rsrc_t mkrs(const void* p, u32 bytes) { return __builtin_amdgcn_make_buffer_rsrc((void*)p, 0, bytes, 0x00020000); }
DEV void dma16(u32 m0v, u32 voff, rsrc_t rs, u32 soff) {
    asm volatile("s_mov_b32 m0, %0\n\ts_nop 0\n\tbuffer_load_dwordx4 %1, %2, %3 offen lds" ::"s"(m0v), "v"(voff), "s"(rs), "s"(soff) : "memory");
}
DEV void dma16n(u32 m0v, u32 voff, rsrc_t rs, u32 soff) {
    asm volatile("s_mov_b32 m0, %0\n\ts_nop 0\n\tbuffer_load_dwordx4 %1, %2, %3 offen nt lds" ::"s"(m0v), "v"(voff), "s"(rs), "s"(soff) : "memory");
}
DEV void st128(u32x4 v, u32 voff, rsrc_t rs, u32 soff) {
    asm volatile("buffer_store_dwordx4 %0, %1, %2, %3 offen sc1\n\ts_nop 1" ::"v"(v), "v"(voff), "s"(rs), "s"(soff) : "memory");
}
DEV void st64(u32x2 v, u32 voff, rsrc_t rs, u32 soff) {
    asm volatile("buffer_store_dwordx2 %0, %1, %2, %3 offen sc1\n\ts_nop 1" ::"v"(v), "v"(voff), "s"(rs), "s"(soff) : "memory");
}
DEV void st32(u32 v, u32 voff, rsrc_t rs) { __builtin_amdgcn_raw_buffer_store_b32(v, rs, voff, 0, 16); }
DEV u32x4 ld128(rsrc_t rs, u32 voff, u32 soff) { return __builtin_amdgcn_raw_buffer_load_b128(rs, voff, soff, 0); }
DEV u32x4 ld128n(rsrc_t rs, u32 voff, u32 soff) { return __builtin_amdgcn_raw_buffer_load_b128(rs, voff, soff, 2); }
DEV u32x4 ld128c(rsrc_t rs, u32 voff, u32 soff) { return __builtin_amdgcn_raw_buffer_load_b128(rs, voff, soff, 16); }
DEV u32x2 ld64(rsrc_t rs, u32 voff, u32 soff) { return __builtin_amdgcn_raw_buffer_load_b64(rs, voff, soff, 0); }
DEV bf16x8 lds128(u32 a) { return *(const AS3 bf16x8*)(uintptr_t)a; }
DEV f32x4 lds128f(u32 a) { return *(const AS3 f32x4*)(uintptr_t)a; }
DEV u32x4 lds128u(u32 a) { return *(const AS3 u32x4*)(uintptr_t)a; }
DEV u32 lds32(u32 a) { return *(const AS3 u32*)(uintptr_t)a; }
DEV void sts128(u32 a, u32x4 v) { *(AS3 u32x4*)(uintptr_t)a = v; }
DEV void sts128f(u32 a, f32x4 v) { *(AS3 f32x4*)(uintptr_t)a = v; }
DEV void sts32(u32 a, u32 v) { *(AS3 u32*)(uintptr_t)a = v; }
DEV bf16x4 ldstr(u32 a) { return __builtin_amdgcn_ds_read_tr16_b64_v4bf16((AS3 bf16x4*)(uintptr_t)a); }
DEV u32x2 ldstru(u32 a) { return __builtin_bit_cast(u32x2, ldstr(a)); }
DEV bf16x8 cat(bf16x4 a, bf16x4 b) { return __builtin_shufflevector(a, b, 0, 1, 2, 3, 4, 5, 6, 7); }
DEV bf16x8 asbf(u32x4 v) { return __builtin_bit_cast(bf16x8, v); }
DEV u32 pk2(float a, float b) { f32x2 f = {a, b}; return __builtin_bit_cast(u32, __builtin_convertvector(f, bf16x2)); }
DEV float bfr(float a) { return __uint_as_float(pk2(a, 0.f) << 16); }
DEV float blo(u32 v) { return __uint_as_float(v << 16); }
DEV float bhi(u32 v) { return __uint_as_float(v & 0xffff0000u); }
#define WAITV(n) asm volatile("s_waitcnt vmcnt(" #n ")" ::: "memory")
#define WAITL() asm volatile("s_waitcnt lgkmcnt(0)" ::: "memory")
#define BARL() asm volatile("s_waitcnt lgkmcnt(0)\n\ts_barrier" ::: "memory")
#define PRIO(n) __builtin_amdgcn_s_setprio(n)
#define SB() __builtin_amdgcn_sched_barrier(0)
#define MFMA16(a, b, c) __builtin_amdgcn_mfma_f32_16x16x32_bf16(a, b, c, 0, 0, 0)
DEV u32 ldsflag1(u32 a) { return *(volatile const AS3 u32*)(uintptr_t)a; }
DEV void ldsflagw(u32 a, u32 v) { *(volatile AS3 u32*)(uintptr_t)a = v; }
DEV u32 lds_add(u32 a) {
    u32 r, one = 1;
    asm volatile("s_waitcnt lgkmcnt(0)\n\ts_mov_b64 exec, 1\n\tds_add_rtn_u32 %0, %1, %2\n\ts_mov_b64 exec, -1\n\ts_waitcnt lgkmcnt(0)" : "=&v"(r) : "v"(a), "v"(one) : "memory");
    return rfl(r);
}
DEV void lds_inc(u32 a) {
    u32 one = 1;
    asm volatile("s_mov_b64 exec, 1\n\tds_add_u32 %0, %1\n\ts_mov_b64 exec, -1" ::"v"(a), "v"(one) : "memory");
}
DEV void lds_wait_ge(u32 have, u32 need, u32 vaddr) {
    u32 vt, cn;
    asm volatile(
        "v_cmp_gt_u32 vcc, %3, %2\n\t"
        "s_cbranch_vccnz Lslow%=\n"
        "Ldone%=:\n\t"
        ".subsection 1\n"
        "Lslow%=:\n\t"
        "s_mov_b32 %1, 0x40000\n"
        "Lloop%=:\n\t"
        "ds_read_b32 %0, %4\n\t"
        "s_sub_u32 %1, %1, 1\n\t"
        "s_cbranch_scc1 Ldone%=\n\t"
        "s_waitcnt lgkmcnt(0)\n\t"
        "v_cmp_gt_u32 vcc, %3, %0\n\t"
        "s_cbranch_vccnz Lloop%=\n\t"
        "s_branch Ldone%=\n\t"
        ".subsection 0\n"
        : "=&v"(vt), "=&s"(cn) : "v"(have), "s"(need), "v"(vaddr) : "memory", "scc", "vcc");
}
DEV void lds_spin(u32 vaddr, u32 need) {
    u32 vt, cn;
    asm volatile(
        "s_mov_b32 %1, 0x40000\n"
        "Lsp%=:\n\t"
        "ds_read_b32 %0, %2\n\t"
        "s_waitcnt lgkmcnt(0)\n\t"
        "v_cmp_gt_u32 vcc, %3, %0\n\t"
        "s_cbranch_vccz Lse%=\n\t"
        "s_sleep 1\n\t"
        "s_sub_u32 %1, %1, 1\n\t"
        "s_cbranch_scc0 Lsp%=\n"
        "Lse%=:"
        : "=&v"(vt), "=&s"(cn) : "v"(vaddr), "s"(need) : "memory", "scc", "vcc");
}
DEV void lds_spinb(u32 vaddr) {
    u32 vt, cn;
    asm volatile(
        "s_mov_b32 %1, 0x20000\n"
        "Lsp%=:\n\t"
        "ds_read_u8 %0, %2\n\t"
        "s_waitcnt lgkmcnt(0)\n\t"
        "v_cmp_eq_u32 vcc, 0, %0\n\t"
        "s_cbranch_vccz Lse%=\n\t"
        "s_sleep 2\n\t"
        "s_sub_u32 %1, %1, 1\n\t"
        "s_cbranch_scc0 Lsp%=\n"
        "Lse%=:"
        : "=&v"(vt), "=&s"(cn) : "v"(vaddr) : "memory", "scc", "vcc");
}
DEV void g_poll1(rsrc_t rs, u32 voff, u32 tag) {
    u32 vt, cn;
    asm volatile(
        "s_mov_b32 %1, 0x8000\n"
        "Lgp%=:\n\t"
        "buffer_load_dword %0, %2, %3, 0 offen sc1\n\t"
        "s_waitcnt vmcnt(0)\n\t"
        "v_cmp_ne_u32 vcc, %4, %0\n\t"
        "s_cbranch_vccz Lge%=\n\t"
        "s_mov_b64 exec, vcc\n\t"
        "s_sleep 4\n\t"
        "s_sub_u32 %1, %1, 1\n\t"
        "s_cbranch_scc0 Lgp%=\n"
        "Lge%=:\n\t"
        "s_mov_b64 exec, -1"
        : "=&v"(vt), "=&s"(cn) : "v"(voff), "s"(rs), "s"(tag) : "memory", "scc", "vcc");
}
DEV void g_pollm(rsrc_t rs, u32 voff, u32 tag, u32 la) {
    u32 vt, cn, m;
    asm volatile(
        "s_mov_b32 %1, 0x8000\n"
        "Lgp%=:\n\t"
        "buffer_load_dword %0, %3, %4, 0 offen sc1\n\t"
        "s_waitcnt vmcnt(0)\n\t"
        "v_cmp_ne_u32 vcc, %5, %0\n\t"
        "s_cbranch_vccz Lge%=\n\t"
        "s_nor_b32 %2, vcc_lo, vcc_hi\n\t"
        "v_mov_b32 %0, %2\n\t"
        "ds_write_b32 %6, %0\n\t"
        "s_mov_b64 exec, vcc\n\t"
        "s_sleep 4\n\t"
        "s_sub_u32 %1, %1, 1\n\t"
        "s_cbranch_scc0 Lgp%=\n"
        "Lge%=:\n\t"
        "s_mov_b64 exec, -1"
        : "=&v"(vt), "=&s"(cn), "=&s"(m) : "v"(voff), "s"(rs), "s"(tag), "v"(la) : "memory", "scc", "vcc");
}
DEV void g_poll2(rsrc_t rs, u32 o0, u32 o1, u32 tag) {
    u32 v0, v1, cn;
    asm volatile(
        "s_mov_b32 %2, 0x4000\n"
        "Lgp%=:\n\t"
        "buffer_load_dword %0, %3, %5, 0 offen sc1\n\t"
        "buffer_load_dword %1, %4, %5, 0 offen sc1\n\t"
        "s_waitcnt vmcnt(0)\n\t"
        "v_xor_b32 %0, %6, %0\n\t"
        "v_xor_b32 %1, %6, %1\n\t"
        "v_or_b32 %0, %0, %1\n\t"
        "v_cmp_ne_u32 vcc, 0, %0\n\t"
        "s_cbranch_vccz Lge%=\n\t"
        "s_mov_b64 exec, vcc\n\t"
        "s_sleep 8\n\t"
        "s_sub_u32 %2, %2, 1\n\t"
        "s_cbranch_scc0 Lgp%=\n"
        "Lge%=:\n\t"
        "s_mov_b64 exec, -1"
        : "=&v"(v0), "=&v"(v1), "=&s"(cn) : "v"(o0), "v"(o1), "s"(rs), "s"(tag) : "memory", "scc", "vcc");
}
DEV void g_poll4(rsrc_t rs, u32 o0, u32 o1, u32 o2, u32 o3, u32 tag) {
    u32 v0, v1, v2, v3, cn;
    asm volatile(
        "s_mov_b32 %4, 0x4000\n"
        "Lgp%=:\n\t"
        "buffer_load_dword %0, %5, %9, 0 offen sc1\n\t"
        "buffer_load_dword %1, %6, %9, 0 offen sc1\n\t"
        "buffer_load_dword %2, %7, %9, 0 offen sc1\n\t"
        "buffer_load_dword %3, %8, %9, 0 offen sc1\n\t"
        "s_waitcnt vmcnt(0)\n\t"
        "v_xor_b32 %0, %10, %0\n\t"
        "v_xor_b32 %1, %10, %1\n\t"
        "v_xor_b32 %2, %10, %2\n\t"
        "v_xor_b32 %3, %10, %3\n\t"
        "v_or3_b32 %0, %0, %1, %2\n\t"
        "v_or_b32 %0, %0, %3\n\t"
        "v_cmp_ne_u32 vcc, 0, %0\n\t"
        "s_cbranch_vccz Lge%=\n\t"
        "s_mov_b64 exec, vcc\n\t"
        "s_sleep 12\n\t"
        "s_sub_u32 %4, %4, 1\n\t"
        "s_cbranch_scc0 Lgp%=\n"
        "Lge%=:\n\t"
        "s_mov_b64 exec, -1"
        : "=&v"(v0), "=&v"(v1), "=&v"(v2), "=&v"(v3), "=&s"(cn) : "v"(o0), "v"(o1), "v"(o2), "v"(o3), "s"(rs), "s"(tag) : "memory", "scc", "vcc");
}

#define TS2(i)
struct TSt {};
#define TS(i)
#define TSB(i)
struct Args { const u16 *x, *k, *v, *sk, *hk, *dv, *wg, *wu, *wd; u16* out; char* ws; };

struct KvRegs { u32x2 kk[11], vv[8]; };
DEV u32x2 ld64n(rsrc_t rs, u32 voff, u32 soff) { return __builtin_amdgcn_raw_buffer_load_b64(rs, voff, soff, 2); }
DEV void kv_issue(const Args& A, KvRegs& R, int r32, int cg, int lane) {
    const int r0 = r32 * 32 + (lane >> 4) * 8;
    rsrc_t rk = mkrs(A.k, S * D * 2), rv = mkrs(A.v, S * D * 2);
    const u32 vo = r32 < 64 ? r0 * (D * 2) + cg * 128 + (lane & 15) * 8 : OOB, vp = (r0 && r32 < 64) ? vo - 3 * D * 2 : OOB;
#pragma unroll
    for (int i = 0; i < 11; ++i) R.kk[i] = i < 3 ? ld64n(rk, vp, i * D * 2) : ld64n(rk, vo, (i - 3) * D * 2);
#pragma unroll
    for (int i = 0; i < 8; ++i) R.vv[i] = ld64n(rv, vo, i * D * 2);
}
DEV void kv_finish(const KvRegs& R, const u32x4 (&w)[2], rsrc_t rw, int r32, int cg, int lane) {
    const u32 so = r32 < 64 ? (lane >> 2) * 4 * 4096 + (2016 - r32 * 32) * 2 + (lane & 3) * 16 : OOB, pl = ((3 - (lane & 3)) * 16 + (lane >> 2)) * 4;
    u32x4 p[4];
#pragma unroll
    for (int c = 0; c < 4; ++c) {
        const u32 wa = w[c >> 1][(c & 1) * 2], wb = w[c >> 1][(c & 1) * 2 + 1];
        const float w0 = blo(wa), w1 = bhi(wa), w2 = blo(wb), w3 = bhi(wb);
        float kf[11], o[8];
#pragma unroll
        for (int i = 0; i < 11; ++i) kf[i] = (c & 1) ? bhi(R.kk[i][c >> 1]) : blo(R.kk[i][c >> 1]);
#pragma unroll
        for (int i = 0; i < 8; ++i) {
            o[i] = __builtin_fmaf(kf[i + 3], w0, __builtin_fmaf(kf[i + 2], w1, __builtin_fmaf(kf[i + 1], w2, kf[i] * w3))) * ((c & 1) ? bhi(R.vv[i][c >> 1]) : blo(R.vv[i][c >> 1]));
            asm("" : "+v"(o[i]));
        }
#pragma unroll
        for (int j = 0; j < 4; ++j) p[c][j] = __builtin_amdgcn_ds_bpermute(pl, pk2(o[7 - 2 * j], o[6 - 2 * j]));
    }
#pragma unroll
    for (int c = 0; c < 4; ++c) st128(p[c], so, rw, WS_KV + (cg * 64 + c) * 4096);
}
DEV void wd_issue(const Args& A, int pu, int h0, int h1, u32 lb, int lane) {
    const int s = pu / 5, l2 = pu - s * 5;
    rsrc_t rd = mkrs(A.wd, DF * D * 2);
    const u32 vo = (32 * s + (lane >> 3)) * (D * 2) + l2 * 256 + (lane & 7) * 16;
#pragma unroll 1
    for (int h = h0; h < h1; ++h)
#pragma unroll
        for (int i = 0; i < 4; ++i) dma16n(lb + h * 4096 + i * 1024, vo, rd, i * 8 * D * 2 + h * 128);
}
DEV void wd_finish(rsrc_t rw, int pu, int h0, int h1, u32 lb, int lane) {
    const int s = pu / 5, l2 = pu - s * 5;
#pragma unroll 1
    for (int h = h0; h < h1; ++h) {
        const u32 ta = lb + h * 4096 + (lane >> 2) * 128 + (lane & 3) * 8;
        u32x2 a[4], b[4];
#pragma unroll
        for (int f = 0; f < 4; ++f) { a[f] = ldstru(ta + f * 32); b[f] = ldstru(ta + f * 32 + 2048); }
#pragma unroll
        for (int f = 0; f < 4; ++f) st128(u32x4{a[f][0], a[f][1], b[f][0], b[f][1]}, lane * 16, rw, WS_WD + ((8 * l2 + 4 * h + f) * 54 + s) * 1024);
    }
}
template <int CTRL> DEV float dppadd(float v) { return v + __uint_as_float(__builtin_amdgcn_update_dpp(0u, __float_as_uint(v), CTRL, 0xf, 0xf, true)); }
DEV u32 row_stats(const u32x4 (&v)[12], int lane) {
    const bf16x8 one = asbf(u32x4{0x3F803F80u, 0x3F803F80u, 0x3F803F80u, 0x3F803F80u});
    float s1[2], s2[2];
#pragma unroll
    for (int a = 0; a < 2; ++a) {
        f32x4 q = {0.f, 0.f, 0.f, 0.f}, s = q;
#pragma unroll
        for (int i = 0; i < 5; ++i) { q = MFMA16(asbf(v[a * 5 + i]), asbf(v[a * 5 + i]), q); s = MFMA16(one, asbf(v[a * 5 + i]), s); }
        const float t = (lane & 2) ? ((lane & 1) ? q[3] : q[2]) : ((lane & 1) ? q[1] : q[0]);
        s2[a] = dppadd<0x4E>(dppadd<0xB1>(t)); s1[a] = dppadd<0x4E>(dppadd<0xB1>(s[0]));
    }
    const float mean = ((lane & 1) ? s1[1] : s1[0]) * (1.f / D);
    const float var = __builtin_fmaxf(__builtin_fmaf(-mean, mean, ((lane & 1) ? s2[1] : s2[0]) * (1.f / D)), 0.f);
    const float den = __builtin_sqrtf(bfr(bfr(var) + 1e-5f));
    return pk2(mean, den);
}
DEV void st_gather(rsrc_t rw, int g0, u32 tag, int lane) {
    u32x4 sv[2];
    const u32 o0 = WS_ST + min(g0 + lane * 2, S - 2) * 8, o1 = WS_ST + min(g0 + 128 + min(lane, 49) * 2, S - 2) * 8;
#pragma unroll 1
    for (int it = 0; it < 0x4000; ++it) {
        asm volatile("" ::: "memory");
        sv[0] = ld128c(rw, o0, 0); sv[1] = ld128c(rw, o1, 0);
        const u32 bad = (sv[0][1] ^ tag) | (sv[0][3] ^ tag) | (sv[1][1] ^ tag) | (sv[1][3] ^ tag);
        if (__builtin_amdgcn_ballot_w64(bad != 0u) == 0) break;
        __builtin_amdgcn_s_sleep(12);
    }
    asm volatile("" ::: "memory");
    const float K1 = -2.f * 1.4426950408889634f * 0.7978845608028654f;
#pragma unroll
    for (int i = 0; i < 2; ++i)
#pragma unroll
        for (int j = 0; j < 2; ++j) {
            const float m = blo(sv[i][2 * j]), den = bhi(sv[i][2 * j]);
            const float r = __builtin_amdgcn_rcpf(den);
            sts128f(EPI + (i * 128 + lane * 2 + j) * 16, f32x4{-m, K1 * r, K1 * 0.044715f * r * r * r, den * den});
        }
}

DEV int wrapn(int v, int n) { return v >= n ? v - n : v; }
DEV int g2_step(int off, int w, int i) { return wrapn(off + (i < 6 ? 8 * i + w : i == 6 ? (w == 2 ? 47 : 52 + (w & 1)) : 48 + w), 54); }
DEV u32 a_area(int w) { return w < 2 ? w * 20480 : 112896 + (w - 2) * 20480; }

template <int TH> DEV void g2_ldw6(bf16x8 (&wr)[6][5], rsrc_t rw, u32 wsrc, int off, int wid, int lane, int j0, int j1) {
#pragma unroll
    for (int j = j0; j < j1; ++j) {
        wr[j / 5][j % 5] = asbf(ld128c(rw, (j >= 25 && wid == 7) ? OOB : (u32)lane * 16, wsrc + ((j % 5) * 54 + g2_step(off, wid, j / 5)) * 1024));
        if (j >= TH && j < 29) asm volatile("s_waitcnt vmcnt(%0)" ::"n"(TH < 63 ? TH : 63) : "memory");
    }
}
DEV void g1_compute(bool busy, bf16x8 (&wr)[6][5], u32 wsrc, int off, int c, int lane, rsrc_t rw, int rt, int grp, TSt& T) {
    const int wi = c >> 1, wj = c & 1;
    const int tok = lane & 15, kq = lane >> 4;
    u32 xa[2], xe[2], wa[2];
    int xm = tok < 8 ? 1 : 0;
    {
        int q = (lane >> 2) & 3, p = lane & 3;
        int f = ((q >> 1) & 1) | ((kq & 1) << 1);
#pragma unroll
        for (int e = 0; e < 2; ++e) {
            wa[e] = WO + 2 * WSZ + (8 * kq + q) * 128 + (((2 * wj + (wi ^ e)) ^ f) << 5) + p * 8;
            u32 t = XS + tok * 128 + (((4 * e + kq) ^ (tok >> 1)) << 4);
            xa[e] = t + wi * 7 * 2048; xe[e] = tok < 8 ? t + 14 * 2048 : ONES;
        }
    }
    f32x4 acc[8][4];
#pragma unroll
    for (int b = 0; b < 8; ++b)
#pragma unroll
        for (int u = 0; u < 4; ++u) acc[b][u] = f32x4{0.f, 0.f, 0.f, 0.f};
    bf16x8 xf[2][8], wf[2][4];
    auto ldf = [&](int ks) {
#pragma unroll
        for (int u = 0; u < 4; ++u) { u32 a = wa[u & 1] + ks * 4096 + (u >> 1) * 8192; wf[ks][u] = cat(ldstr(a), ldstr(a + 512)); }
#pragma unroll
        for (int b = 0; b < 7; ++b) xf[ks][b] = lds128(xa[ks] + b * 2048);
        xf[ks][7] = lds128(xe[ks]);
    };
    auto mm7 = [&](int ks) {
        acc[7][0] = MFMA16(wf[ks][0], xf[ks][7], acc[7][0]);
        acc[7][2] = MFMA16(wf[ks][2], xf[ks][7], acc[7][2]);
    };
    auto mm = [&](int ks, int b0, int b1) {
#pragma unroll
        for (int b = b0; b < b1; ++b)
#pragma unroll
            for (int u = 0; u < 4; ++u) acc[b][u] = MFMA16(wf[ks][u], xf[ks][b], acc[b][u]);
    };
    auto pin = [&]() {
#pragma unroll
        for (int b = 0; b < 7; ++b) asm volatile("" : "+v"(acc[b][0]), "+v"(acc[b][1]), "+v"(acc[b][2]), "+v"(acc[b][3]));
        asm volatile("" : "+v"(acc[7][0]), "+v"(acc[7][2]));
        SB();
    };
    if (busy) {
    lds_spin(L_CNT0, 4);
    TS(1);
    u32 fa = L_FULL + (lane & 1) * 4, fw = L_FREE + c * 4, cnt = 1;
    asm volatile("" : "+v"(fa), "+v"(fw), "+v"(cnt), "+v"(xm));
    int xs = 1, wsl = 2;
    u32 have;
    auto halfA = [&]() {
        ldf(1);
        ldsflagw(fw, cnt);
        have = ldsflag1(fa);
        const int dx = xs == 2 ? -2 * (int)XS : (int)XS, dw = wsl == 3 ? -3 * (int)WSZ : (int)WSZ;
        xs = xs == 2 ? 0 : xs + 1; wsl = (wsl + 1) & 3;
        mm(0, 0, 7); mm7(0);
#pragma unroll
        for (int e = 0; e < 2; ++e) { xa[e] += dx; xe[e] += __mul24(xm, dx); wa[e] += dw; }
        cnt += 1;
#pragma unroll
        for (int i = 0; i < 16; ++i) { __builtin_amdgcn_sched_group_barrier(0x008, 1, 0); __builtin_amdgcn_sched_group_barrier(0x100, 1, 0); }
        __builtin_amdgcn_sched_group_barrier(0x008, 1, 0); __builtin_amdgcn_sched_group_barrier(0x200, 1, 0);
        __builtin_amdgcn_sched_group_barrier(0x008, 1, 0); __builtin_amdgcn_sched_group_barrier(0x100, 1, 0);
#pragma unroll
        for (int i = 0; i < 8; ++i) { __builtin_amdgcn_sched_group_barrier(0x008, 1, 0); __builtin_amdgcn_sched_group_barrier(0x002, 1, 0); }
        __builtin_amdgcn_sched_group_barrier(0x008, 4, 0);
        asm volatile("" : "+v"(xa[0]), "+v"(xa[1]), "+v"(xe[0]), "+v"(xe[1]), "+v"(wa[0]), "+v"(wa[1]), "+v"(cnt), "+v"(have));
        pin();
    };
    ldf(0);
    pin();
    halfA();
#pragma unroll 1
    for (int st = 0; st < 9; ++st) {
        lds_wait_ge(have, st + 1, fa);
        ldf(0);
        mm(1, 0, 7); mm7(1);
#pragma unroll
        for (int i = 0; i < 16; ++i) { __builtin_amdgcn_sched_group_barrier(0x008, 1, 1); __builtin_amdgcn_sched_group_barrier(0x100, 1, 1); }
        __builtin_amdgcn_sched_group_barrier(0x008, 14, 1);
        pin();
        halfA();
    }
    mm7(1); mm(1, 0, 2);
    SB();
    if (tok == 8) {
        sts128f(CSX + (2 * wj + wi) * 64 + kq * 16, acc[7][0]);
        sts128f(CSX + 256 + (2 * wj + wi) * 64 + kq * 16, acc[7][2]);
    }
    lds_inc(L_CS + wj * 4);
    SB();
    mm(1, 2, 7);
    pin();
    TS(2);
    lds_spin(lane & 1 ? L_EPI : L_CS + wj * 4, 2);
    }
    lds_spin(L_P0ALL, 1);
    g2_ldw6<99>(wr, rw, wsrc, off, c, lane, 0, WG0);
    SB();
    TS(3);
    f32x4 cs[4];
#pragma unroll
    for (int u = 0; u < 4; ++u) cs[u] = lds128f(CSX + (u >> 1) * 256 + (2 * wj + (wi ^ (u & 1))) * 64 + kq * 16);
    auto act2 = [&](const f32x4& e, const f32x4& g, const f32x4& u, const f32x4& cg, const f32x4& cu, int i) {
        const f32x2 e0 = {e[0], e[0]}, e1 = {e[1], e[1]}, e2 = {e[2], e[2]}, e3 = {e[3], e[3]};
        const f32x2 a = __builtin_elementwise_fma(e0, f32x2{cg[i], cg[i + 1]}, f32x2{g[i], g[i + 1]});
        const f32x2 b = __builtin_elementwise_fma(e0, f32x2{cu[i], cu[i + 1]}, f32x2{u[i], u[i + 1]});
        const f32x2 z = a * __builtin_elementwise_fma(a * a, e2, e1);
        const f32x2 ex = {__builtin_amdgcn_exp2f(z[0]), __builtin_amdgcn_exp2f(z[1])};
        const f32x2 d = __builtin_elementwise_fma(ex, e3, e3);
        const f32x2 r = (a * b) * f32x2{__builtin_amdgcn_rcpf(d[0]), __builtin_amdgcn_rcpf(d[1])};
        return pk2(r[0], r[1]);
    };
    const int gl = ((rt * RT) & 15) + tok;
    const u32 hv = busy ? (gl >> 4) * (54 * 1024) + (kq * 16 + (gl & 15)) * 16 : OOB;
    const u32 hbase = WS_H + ((((rt * RT) >> 4) + wi * 7) * 54 + grp * 2 + wj) * 1024;
#pragma unroll
    for (int b = 0; b < 7; ++b) {
        f32x4 e = lds128f(EPI + ((wi * 7 + b) * 16 + tok) * 16);
        u32 o[4];
#pragma unroll
        for (int h = 0; h < 2; ++h) { o[2 * h] = act2(e, acc[b][h], acc[b][2 + h], cs[h], cs[2 + h], 0); o[2 * h + 1] = act2(e, acc[b][h], acc[b][2 + h], cs[h], cs[2 + h], 2); }
        u32x4 ov = wi ? u32x4{o[2], o[3], o[0], o[1]} : u32x4{o[0], o[1], o[2], o[3]};
        st128(ov, hv, rw, hbase + b * 54 * 1024);
        if (b < 6) { g2_ldw6<99>(wr, rw, wsrc, off, c, lane, WG0 + b * WGS, WG0 + (b + 1) * WGS); SB(); }
        if (b == GATE) *(volatile AS3 unsigned char*)(uintptr_t)(L_GD + c) = 1;
    }
    if (rt < 8) {
        f32x4 e = lds128f(EPI + (14 * 16 + tok) * 16);
        u32x2 ov = {act2(e, acc[7][0], acc[7][2], cs[0], cs[2], 0), act2(e, acc[7][0], acc[7][2], cs[0], cs[2], 2)};
        st64(ov, tok < 4 ? hv : OOB, rw, hbase + (14 - wi * 7) * 54 * 1024 + wi * 8);
    }
}
struct G1Src { rsrc_t rx, rg, ru; u32 xv0, xv1, xl, wv0, wv1; int ro; };
DEV void g1_loader(int e, const G1Src& g, int lane, rsrc_t rw) {
    const u32 fr = L_FREE + (lane & 3) * 4, fu = L_FULL + e * 4;
    const u32 xv = e ? g.xv1 : g.xv0, wv = e ? g.wv1 : g.wv0;
    auto ld_w = [&](int s) {
        const u32 so = WO + ((s + 2) & 3) * WSZ + e * 1024, ks = wrapn(s + g.ro, 10) * (64 * DF * 2) + e * 8 * DF * 2;
#pragma unroll
        for (int p = 0; p < 16; p += 2) dma16(so + p * 1024, wv, (p >> 3) ? g.ru : g.rg, ks + (p & 7) * 8 * DF * 2);
    };
    ld_w(1);
    int xs = 2;
#pragma unroll 1
    for (int s = 1; s < 10; ++s) {
        if (s > 2 && __builtin_amdgcn_ballot_w64(ldsflag1(fr) < (u32)(s - 2)) != 0) {
            WAITV(8);
            ldsflagw(fu, s - 1);
            lds_spin(fr, s - 2);
        }
        TS2(s);
        const u32 so = xs * XS + e * 1024, ks = wrapn(s + g.ro, 10) * 128 + e * 8 * D * 2;
        xs = xs == 2 ? 0 : xs + 1;
#pragma unroll
        for (int p = 0; p < 28; p += 2) {
            dma16(so + p * 1024, xv, g.rx, ks + p * 8 * D * 2);
            if (p == 14) { WAITV(16); ldsflagw(fu, s - 1); }
        }
        if (!e) dma16(so + 28 * 1024, g.xl, g.rx, ks + 28 * 8 * D * 2);
        if (s < 9) ld_w(s + 1);
    }
    lds_inc(L_LDONE);
    WAITV(0);
    ldsflagw(fu, 9);
    TS2(10);
}

DEV void conv_build(const u32x4 (&v)[4], u32 ib, int lane) {
    const u32 ua = ib + (lane >> 4) * 320 + (lane & 15) * 16;
#pragma unroll
    for (int i = 0; i < 4; ++i) {
        u32 sel = lane == 0 ? (i < 3 ? v[i < 3 ? i + 1 : 0][0] : 0u) : v[i][0];
        u32 nx = __builtin_amdgcn_ds_bpermute(((lane + 1) & 63) * 4, sel);
        sts128(ua + IMG_U0 + i * 1280, v[i]);
        sts128(ua + IMG_U1 + i * 1280, u32x4{__builtin_amdgcn_alignbit(v[i][1], v[i][0], 16), __builtin_amdgcn_alignbit(v[i][2], v[i][1], 16),
                                             __builtin_amdgcn_alignbit(v[i][3], v[i][2], 16), __builtin_amdgcn_alignbit(nx, v[i][3], 16)});
    }
    if (lane < 36) sts128(lane < 32 ? ib + (lane < 16 ? IMG_U0 : IMG_U1) + 5120 + (lane & 15) * 16 : ib + (lane - 32) * 16, u32x4{0u, 0u, 0u, 0u});
}
DEV void conv_task(int g, float dv, u32 ib, u32 yst, int lane, TSt& T) {
    const int a = lane & 15, q = lane >> 4;
    u32 ha = ib + 4 + 4 * a + 16 * q;
    const u32 ub = ib + ((a & 1) ? IMG_U0 : IMG_U1) + 16 * q;
    u32 ua = ub + (15 - 8 * g - (a >> 1)) * 320, un;
    const u32 uz = ub + 5120;
    f32x4 acc[4];
#pragma unroll
    for (int j = 0; j < 8; ++j) {
        const int e = 2046 - 1024 * g - (32 * (j >> 1) + 2 * a + 128 * (2 * q + (j & 1)));
        const u32 uu = lds32(ib + IMG_U0 + (e >> 7) * 320 + (e & 127) * 2);
        acc[j >> 1][2 * (j & 1)] = bhi(uu) * dv; acc[j >> 1][2 * (j & 1) + 1] = blo(uu) * dv;
    }
    u32 nit = 4 * g + 3;
    asm volatile(
        "ds_read_b128 v[240:243], %4 offset:0\n\t"
        "ds_read2_b32 v[208:209], %6 offset0:0 offset1:1\n\t"
        "ds_read2_b32 v[210:211], %6 offset0:2 offset1:3\n\t"
        "ds_read_b128 v[244:247], %4 offset:64\n\t"
        "ds_read2_b32 v[212:213], %6 offset0:16 offset1:17\n\t"
        "ds_read2_b32 v[214:215], %6 offset0:18 offset1:19\n\t"
        "ds_read_b128 v[248:251], %4 offset:128\n\t"
        "ds_read2_b32 v[216:217], %6 offset0:32 offset1:33\n\t"
        "ds_read2_b32 v[218:219], %6 offset0:34 offset1:35\n\t"
        "v_mov_b32 v228, 0\n\t"
        "v_mov_b32 v229, 0\n\t"
        "v_mov_b32 v230, 0\n\t"
        "v_mov_b32 v231, 0\n\t"
        "v_mov_b32 v232, 0\n\t"
        "v_mov_b32 v233, 0\n\t"
        "v_mov_b32 v234, 0\n\t"
        "v_mov_b32 v235, 0\n\t"
        "v_mov_b32 v236, 0\n\t"
        "v_mov_b32 v237, 0\n\t"
        "v_mov_b32 v238, 0\n\t"
        "v_mov_b32 v239, 0\n"
        "Lcv%=:\n\t"
        "s_waitcnt lgkmcnt(6)\n\t"
        "v_mfma_f32_16x16x32_bf16 %0, v[240:243], v[228:231], %0\n\t"
        "ds_read_b128 v[252:255], %4 offset:192\n\t"
        "v_mfma_f32_16x16x32_bf16 %1, v[240:243], v[232:235], %1\n\t"
        "ds_read2_b32 v[220:221], %6 offset0:48 offset1:49\n\t"
        "v_mfma_f32_16x16x32_bf16 %2, v[240:243], v[236:239], %2\n\t"
        "ds_read2_b32 v[222:223], %6 offset0:50 offset1:51\n\t"
        "v_add_u32 %5, 0x140, %4\n\t"
        "v_mfma_f32_16x16x32_bf16 %3, v[240:243], v[208:211], %3\n\t"
        "v_min_u32 %5, %5, %8\n\t"
        "s_waitcnt lgkmcnt(6)\n\t"
        "v_mfma_f32_16x16x32_bf16 %0, v[244:247], v[232:235], %0\n\t"
        "ds_read_b128 v[240:243], %5 offset:0\n\t"
        "v_mfma_f32_16x16x32_bf16 %1, v[244:247], v[236:239], %1\n\t"
        "ds_read2_b32 v[224:225], %6 offset0:64 offset1:65\n\t"
        "v_mfma_f32_16x16x32_bf16 %2, v[244:247], v[208:211], %2\n\t"
        "ds_read2_b32 v[226:227], %6 offset0:66 offset1:67\n\t"
        "v_mfma_f32_16x16x32_bf16 %3, v[244:247], v[212:215], %3\n\t"
        "s_waitcnt lgkmcnt(6)\n\t"
        "v_mfma_f32_16x16x32_bf16 %0, v[248:251], v[236:239], %0\n\t"
        "ds_read_b128 v[244:247], %5 offset:64\n\t"
        "v_mfma_f32_16x16x32_bf16 %1, v[248:251], v[208:211], %1\n\t"
        "ds_read2_b32 v[228:229], %6 offset0:80 offset1:81\n\t"
        "v_mfma_f32_16x16x32_bf16 %2, v[248:251], v[212:215], %2\n\t"
        "ds_read2_b32 v[230:231], %6 offset0:82 offset1:83\n\t"
        "v_add_u32 %4, 0x140, %5\n\t"
        "v_mfma_f32_16x16x32_bf16 %3, v[248:251], v[216:219], %3\n\t"
        "v_min_u32 %4, %4, %8\n\t"
        "s_waitcnt lgkmcnt(6)\n\t"
        "v_mfma_f32_16x16x32_bf16 %0, v[252:255], v[208:211], %0\n\t"
        "ds_read_b128 v[248:251], %5 offset:128\n\t"
        "v_mfma_f32_16x16x32_bf16 %1, v[252:255], v[212:215], %1\n\t"
        "ds_read2_b32 v[232:233], %6 offset0:96 offset1:97\n\t"
        "v_mfma_f32_16x16x32_bf16 %2, v[252:255], v[216:219], %2\n\t"
        "ds_read2_b32 v[234:235], %6 offset0:98 offset1:99\n\t"
        "v_mfma_f32_16x16x32_bf16 %3, v[252:255], v[220:223], %3\n\t"
        "s_waitcnt lgkmcnt(6)\n\t"
        "v_mfma_f32_16x16x32_bf16 %0, v[240:243], v[212:215], %0\n\t"
        "ds_read_b128 v[252:255], %5 offset:192\n\t"
        "v_mfma_f32_16x16x32_bf16 %1, v[240:243], v[216:219], %1\n\t"
        "ds_read2_b32 v[236:237], %6 offset0:112 offset1:113\n\t"
        "v_mfma_f32_16x16x32_bf16 %2, v[240:243], v[220:223], %2\n\t"
        "ds_read2_b32 v[238:239], %6 offset0:114 offset1:115\n\t"
        "v_mfma_f32_16x16x32_bf16 %3, v[240:243], v[224:227], %3\n\t"
        "s_waitcnt lgkmcnt(6)\n\t"
        "v_mfma_f32_16x16x32_bf16 %0, v[244:247], v[216:219], %0\n\t"
        "ds_read_b128 v[240:243], %4 offset:0\n\t"
        "v_mfma_f32_16x16x32_bf16 %1, v[244:247], v[220:223], %1\n\t"
        "ds_read2_b32 v[208:209], %6 offset0:128 offset1:129\n\t"
        "v_mfma_f32_16x16x32_bf16 %2, v[244:247], v[224:227], %2\n\t"
        "ds_read2_b32 v[210:211], %6 offset0:130 offset1:131\n\t"
        "v_mfma_f32_16x16x32_bf16 %3, v[244:247], v[228:231], %3\n\t"
        "s_waitcnt lgkmcnt(6)\n\t"
        "v_mfma_f32_16x16x32_bf16 %0, v[248:251], v[220:223], %0\n\t"
        "ds_read_b128 v[244:247], %4 offset:64\n\t"
        "v_mfma_f32_16x16x32_bf16 %1, v[248:251], v[224:227], %1\n\t"
        "ds_read2_b32 v[212:213], %6 offset0:144 offset1:145\n\t"
        "v_mfma_f32_16x16x32_bf16 %2, v[248:251], v[228:231], %2\n\t"
        "ds_read2_b32 v[214:215], %6 offset0:146 offset1:147\n\t"
        "v_mfma_f32_16x16x32_bf16 %3, v[248:251], v[232:235], %3\n\t"
        "s_sub_u32 %7, %7, 1\n\t"
        "s_waitcnt lgkmcnt(6)\n\t"
        "v_mfma_f32_16x16x32_bf16 %0, v[252:255], v[224:227], %0\n\t"
        "ds_read_b128 v[248:251], %4 offset:128\n\t"
        "v_mfma_f32_16x16x32_bf16 %1, v[252:255], v[228:231], %1\n\t"
        "ds_read2_b32 v[216:217], %6 offset0:160 offset1:161\n\t"
        "v_mfma_f32_16x16x32_bf16 %2, v[252:255], v[232:235], %2\n\t"
        "ds_read2_b32 v[218:219], %6 offset0:162 offset1:163\n\t"
        "v_mfma_f32_16x16x32_bf16 %3, v[252:255], v[236:239], %3\n\t"
        "v_add_u32 %6, 0x200, %6\n\t"
        "s_cbranch_scc0 Lcv%=\n\t"
        "s_waitcnt lgkmcnt(0)\n\t"
        "s_nop 7\n\t"
        "s_nop 7\n\t"
        : "+v"(acc[0]), "+v"(acc[1]), "+v"(acc[2]), "+v"(acc[3]), "+v"(ua), "=&v"(un), "+v"(ha), "+s"(nit)
        : "v"(uz)
        : "memory", "scc", "v208", "v209", "v210", "v211", "v212", "v213", "v214", "v215", "v216", "v217", "v218", "v219", "v220", "v221", "v222", "v223", "v224", "v225", "v226", "v227", "v228", "v229", "v230", "v231", "v232", "v233", "v234", "v235", "v236", "v237", "v238", "v239", "v240", "v241", "v242", "v243", "v244", "v245", "v246", "v247", "v248", "v249", "v250", "v251", "v252", "v253", "v254", "v255");
    TS(10);
#pragma unroll
    for (int j = 0; j < 8; ++j) sts32(yst + (32 * (j >> 1) + 2 * a + 128 * (2 * q + (j & 1))) * 2, pk2(acc[j >> 1][2 * (j & 1)], acc[j >> 1][2 * (j & 1) + 1]));
    asm volatile("" ::: "memory");
}
DEV void y_out(rsrc_t rw, u32 yst, u32 ya, u32 vo, int lane) {
    const u32x4 y0 = lds128u(yst + lane * 16), y1 = lds128u(yst + 1024 + lane * 16);
    st128(y0, vo, rw, ya);
    st128(y1, vo, rw, ya + 1024);
}

DEV void g2_ldw(bf16x8 (&wr)[5], rsrc_t rw, u32 vo, u32 wsrc, int ks) {
#pragma unroll
    for (int f = 0; f < 5; ++f) wr[f] = asbf(ld128c(rw, vo, wsrc + (f * 54 + ks) * 1024));
}
DEV void mm5_opt(f32x4 (&c)[5], const bf16x8 (&a)[5], bf16x8 h, int w) {
    asm volatile(
        "s_cmp_eq_u32 %11, 3\n\t"
        "s_cbranch_scc1 Lmo%=\n\t"
        "v_mfma_f32_16x16x32_bf16 %0, %5, %10, %0\n\t"
        "v_mfma_f32_16x16x32_bf16 %1, %6, %10, %1\n\t"
        "v_mfma_f32_16x16x32_bf16 %2, %7, %10, %2\n\t"
        "v_mfma_f32_16x16x32_bf16 %3, %8, %10, %3\n\t"
        "v_mfma_f32_16x16x32_bf16 %4, %9, %10, %4\n"
        "Lmo%=:"
        : "+v"(c[0]), "+v"(c[1]), "+v"(c[2]), "+v"(c[3]), "+v"(c[4]) : "v"(a[0]), "v"(a[1]), "v"(a[2]), "v"(a[3]), "v"(a[4]), "v"(h), "s"(w) : "scc");
}
DEV void g2_fin(rsrc_t rw, const bf16x8 (&wr)[6][5], const bf16x8 (&wx)[2][5], int w, int off, int blk, int lane, TSt& T) {
    f32x4 acc[4][5];
#pragma unroll
    for (int t = 0; t < 4; ++t)
#pragma unroll
        for (int f = 0; f < 5; ++f) acc[t][f] = f32x4{0.f, 0.f, 0.f, 0.f};
    bf16x8 hf[32];
    const u32 vz = w == 3 ? OOB : (u32)lane * 16;
    const u32 la = a_area(w) + lane * 16;
    auto ldh = [&](int n) { hf[n] = asbf(ld128c(rw, (n & 7) == 6 ? vz : (u32)lane * 16, WS_H + ((blk + (((n >> 3) + 1 + w) & 3)) * 54 + g2_step(off, w, n & 7)) * 1024)); };
#pragma unroll
    for (int n = 0; n < LKA; ++n) ldh(n);
    SB();
#pragma unroll
    for (int n = 0; n < 32; ++n) {
        const int t = n >> 3, i = n & 7;
        if (SKIP && i == 6) mm5_opt(acc[t], wx[0], hf[n], w);
        else {
#pragma unroll
            for (int f = 0; f < 5; ++f) acc[t][f] = MFMA16(i < 6 ? wr[i < 6 ? i : 0][f] : wx[i & 1][f], hf[n], acc[t][f]);
        }
        if (n + LKA < 32) ldh(n + LKA);
        if (t > 0 && i < 5) sts128f(la + ((t - 1) * 5 + i) * 1024, acc[t > 0 ? t - 1 : 0][i < 5 ? i : 0]);
        if (n == 29) lds_inc(L_AF);
        SB();
    }
    TS(7);
    lds_spin(L_AS, 4);
#pragma unroll
    for (int f = 0; f < 5; ++f) sts128f(la + (15 + f) * 1024, acc[3][f]);
    ldsflagw(L_FREE + w * 4, 11u);
}
DEV void g2_pre(const Args& A, rsrc_t rw, const bf16x8 (&wr)[6][5], int w, int off, int blk, int cgp, u32 tag, u32 sv, u32 f2, u32 ya, int lane, TSt& T) {
    f32x4 acc[4][5];
#pragma unroll
    for (int t = 0; t < 4; ++t)
#pragma unroll
        for (int f = 0; f < 5; ++f) acc[t][f] = f32x4{0.f, 0.f, 0.f, 0.f};
    bf16x8 hf[24];
    u32x4 yv[3];
    u32x2 xv[5];
    u32 yf = 0, yok = 0;
    const u32 yfa = WS_F2 + (cgp * 32 + (lane & 31)) * F2S, yo = (lane >> 3) * 4096 + (lane & 7) * 16;
    auto ldy = [&](u32 vo) {
#pragma unroll
        for (int j = 0; j < 3; ++j) yv[j] = ld128c(rw, (j == 2 && w >= 2) ? OOB : vo, WS_Y + (cgp * 80 + (j * 4 + w) * 8) * 4096 + blk * 32);
    };
    const u32 vz = w == 3 ? OOB : (u32)lane * 16;
    auto ldh = [&](int n) { hf[n] = asbf(ld128c(rw, n % 6 == 5 ? vz : (u32)lane * 16, WS_H + ((blk + ((n / 6 + 1 + w) & 3)) * 54 + g2_step(off, w + 4, n % 6)) * 1024)); };
    y_out(rw, YST + w * 2048, ya, f2 ? (u32)lane * 16 : OOB, lane);
    lds_inc(L_CD);
    SB();
#pragma unroll
    for (int n = 0; n < LKB; ++n) ldh(n);
    SB();
    asm volatile("s_waitcnt vmcnt(%0)" ::"n"(LKB) : "memory");
    st32(tag, (lds_add(f2 ? L_YCNT : L_NIL) == 3u && lane == 0 && f2) ? f2 : OOB, rw);
#pragma unroll
    for (int f = 0; f < 5; ++f) xv[f] = *(const AS3 u32x2*)(uintptr_t)(a_area(w) + 15 * 1024 + (lane & 15) * 160 + (lane >> 4) * 8 + f * 32);
    lds_inc(L_AS);
    TS(5);
    SB();
#pragma unroll
    for (int n = 0; n < 24; ++n) {
        const int t = n / 6, i = n % 6;
        if (n == 6) lds_spin(L_CD, 4);
        if (SKIP && i == 5) mm5_opt(acc[t], wr[5], hf[n], w);
        else {
#pragma unroll
            for (int f = 0; f < 5; ++f) acc[t][f] = MFMA16(wr[i][f], hf[n], acc[t][f]);
        }
        if (n + LKB < 24) ldh(n + LKB);
        if (t > 0 && i < 5) sts128f(RB + ((((t + w) & 3) * 3 + t - 1) * 5 + i) * 1024 + lane * 16, acc[t > 0 ? t - 1 : 0][i < 5 ? i : 0]);
        if (n == YN) yf = __builtin_amdgcn_raw_buffer_load_b32(rw, w == 2 ? yfa : OOB, 0, 16);
        if (n == YN + YD) ldsflagw(w == 2 ? L_YOK : L_NIL, __builtin_amdgcn_ballot_w64(yf != tag) == 0 ? 1u : 0u);
        if (n == YN + YD + 1) yf = ldsflag1(L_YOK);
        if (n == YN + YD + 2) { yok = rfl(yf); ldy(yok ? yo : OOB); }
        SB();
    }
    asm volatile("s_nop 7\n\ts_nop 7" ::: "memory");
    lds_inc(L_AD);
    TS(7);
    if (!yok) {
        if (w == 2) { g_poll1(rw, yfa, tag); ldsflagw(L_YOK, 1u); }
        else lds_spin(L_YOK, 1);
        ldy(yo);
    }
    TS(8);
    SB();
    f32x4 xn[5], pr[15];
    const u32 ta = RB + w * 15360;
    lds_spin(L_AD, 4);
#pragma unroll
    for (int j = 0; j < 15; ++j) pr[j] = lds128f(ta + j * 1024 + lane * 16);
    {
        const float r = __builtin_amdgcn_rcpf(bhi(sv)), mr = -blo(sv) * r;
#pragma unroll
        for (int f = 0; f < 5; ++f) {
            const f32x4 xx = {blo(xv[f][0]), bhi(xv[f][0]), blo(xv[f][1]), bhi(xv[f][1])};
            acc[3][f] += xx;
            xn[f] = xx * r + mr;
        }
    }
#pragma unroll
    for (int f = 0; f < 5; ++f) acc[3][f] = (acc[3][f] + pr[f]) + (pr[5 + f] + pr[10 + f]);
    SB();
    lds_spin(L_AF, 4);
#pragma unroll
    for (int j = 0; j < 15; ++j) pr[j] = lds128f(a_area((w + 3 - j / 5) & 3) + j * 1024 + lane * 16);
#pragma unroll
    for (int f = 0; f < 5; ++f) acc[3][f] = (acc[3][f] + pr[f]) + (pr[5 + f] + pr[10 + f]);
    SB();
#pragma unroll
    for (int j = 0; j < 3; ++j) if (j < 2 || w < 2) sts128(YT + (j * 4 + w) * 1024 + lane * 16, yv[j]);
    lds_inc(L_YRDY);
    lds_spin(L_YRDY, 4);
#pragma unroll
    for (int f = 0; f < 5; ++f) {
        const u32x2 y = ldstru(YT + (16 * f + (lane >> 2)) * 128 + w * 32 + (lane & 3) * 8);
        acc[3][f] += xn[f] * f32x4{blo(y[0]), bhi(y[0]), blo(y[1]), bhi(y[1])};
    }
    SB();
    lds_spin(L_FREE + w * 4, 11);
    TS(9);
#pragma unroll
    for (int f = 0; f < 5; ++f) pr[f] = lds128f(a_area(w) + (15 + f) * 1024 + lane * 16);
#pragma unroll
    for (int f = 0; f < 5; ++f) {
        const f32x4 o = acc[3][f] + pr[f];
        *(AS3 u32x2*)(uintptr_t)(ta + (lane & 15) * 160 + (lane >> 4) * 8 + f * 32) = u32x2{pk2(o[0], o[1]), pk2(o[2], o[3])};
    }
    asm volatile("" ::: "memory");
    rsrc_t ro = mkrs(A.out, S * D * 2);
#pragma unroll
    for (int j = 0; j < 3; ++j) {
        const int e = j * 64 + lane, r = e / 10;
        __builtin_amdgcn_raw_buffer_store_b128(lds128u(ta + e * 16), ro, e < 160 ? ((blk + w) * 16 + r) * (D * 2) + cgp * 160 + (e - r * 10) * 16 : OOB, 0, OUTAUX);
    }
}

#define P0A 0
#define WPRE 15
#define WPRE2 30
#define IDLET 880u
#define THRB 99
#define POLL7 6
#define POLLAT 6
__global__ __launch_bounds__(512, 2) void fused_kernel(Args A) {
    const int b = blockIdx.x, lane = threadIdx.x & 63, wid = rfl(threadIdx.x >> 6);
    const int xcd = b & 7, idx = b >> 3;
    rsrc_t rw = mkrs(A.ws, WS_SIZE);
    TSt T = {};
    TS(0);
    const int hx = (xcd + 2) >> 3, n4 = xcd * 4, dv = (0x45333333u >> n4) & 15, c = ((0x540u >> xcd) & 32) + idx;
    const int cq = (c * ((0x344056u >> (dv * 8 - 24)) & 255)) >> 8, cr = c - cq * dv, over = c - dv * (21 - 15 * hx);
    const int rt = cr + ((0x50663300u >> n4) & 15), grp = cq + 21 * hx;
    const int idle = (over + ((0x53221100u >> n4) & 15)) | (over >> 31);
    const int tr = cq * (1 + hx) + cr * (3 - hx) + ((0xB0221100u >> n4) & 15);
    int ro = tr - 10 * ((tr * 26) >> 8);
    const bool busy = idle < 0;
    if (threadIdx.x >= 448 && lane < 36) ldsflagw(lane < 32 ? FLG + lane * 4 : ONES + (lane - 32) * 4, lane >= 32 ? 0x3F803F80u : (!busy && lane < 8) ? 10u : 0u);
    G1Src g;
    g.rx = mkrs(A.x, S * D * 2); g.rg = mkrs(A.wg, D * DF * 2); g.ru = mkrs(A.wu, D * DF * 2); g.ro = ro;
    { int row = lane >> 3;
      g.xv0 = (rt * RT + row) * (D * 2) + (((lane & 7) ^ (row >> 1)) << 4); g.xv1 = g.xv0 ^ 64; g.xl = row < 4 ? g.xv0 : OOB;
      g.wv0 = row * (DF * 2) + grp * 128 + (((lane & 7) ^ (((row >> 1) & 1) << 1)) << 4); g.wv1 = g.wv0 ^ 64; }
    if (wid < 4) {
        PRIO(3);
        if (busy) {
            if (wid & 1) { g.xv0 = g.xv1; g.wv0 = g.wv1; g.xl = OOB; }
            const u32 ks = ro * 128 + (wid & 1) * 8 * D * 2, kw = ro * (64 * DF * 2) + (wid & 1) * 8 * DF * 2, lo = (wid & 1) * 1024;
            if (wid < 2) {
#pragma unroll
                for (int p = 0; p < 22; p += 2) dma16(XS + lo + p * 1024, g.xv0, g.rx, ks + p * 8 * D * 2);
            } else {
#pragma unroll
                for (int p = 0; p < 16; p += 2) dma16(WO + 2 * WSZ + lo + p * 1024, g.wv0, (p >> 3) ? g.ru : g.rg, kw + (p & 7) * 8 * DF * 2);
#pragma unroll
                for (int p = 22; p < 28; p += 2) dma16(XS + lo + p * 1024, g.xv0, g.rx, ks + p * 8 * D * 2);
                if (wid == 2) dma16(XS + 28 * 1024, g.xl, g.rx, ks + 28 * 8 * D * 2);
            }
        }
    }
    u32 cpf = 0, ep = 0;
        if (wid == 7) {
            uint64_t pc; u32 len;
            asm volatile("s_getpc_b64 %0\nLcs%=:\n\ts_mov_b32 %1, Lce%=-Lcs%=\n\t.subsection 2\nLce%=:\n\t.subsection 0" : "=s"(pc), "=s"(len));
            cpf = __builtin_amdgcn_raw_buffer_load_b32(mkrs((const void*)pc, min(len, 40960u)), lane < 10 ? (idx * 10 + lane) * 128 : OOB, 0, 0);
        }
        if (wid == 6) ep = __builtin_amdgcn_raw_buffer_load_b32(rw, 0, 0, 16);
    BARL();
    const u32 ts0 = (u32)__builtin_amdgcn_s_memrealtime();
    if (busy && wid < 4) { PRIO(1); WAITV(0);
        lds_inc(L_CNT0); }
    const int pr = (b >> 4) * 8 + xcd, mem = idx & 1;
    const int c0 = pr * 5 + mem * 3;
    u32x4 e[12];
    const u32 fr = L_FREE + (lane & 3) * 4;
    if (!busy || wid >= 6) {
        const int q = rt * 27 + grp;
        int kvt = 320, hf = 0, nkv = 1, wdp = -1, stu = -1;
        int h0 = 0, h1 = 0;
        if (busy) { kvt = q; hf = wid - 6; wdp = q; h0 = 1 - hf; h1 = 2 - hf; stu = q + q / 18; }
        else {
            if (wid < 4) { kvt = 243 + idle * 6 + wid; nkv = 2; }
            else { kvt = 245 + idle * 6 + (wid >> 1); hf = wid & 1; wdp = 241 + 2 * idle + (wid >> 1); h0 = hf; h1 = hf + 1; }
            stu = 19 * idle + 18;
            if (kvt == 320) { nkv = 0; wdp = 268 + hf; h0 = 0; h1 = 2; }
        }
        const int r32 = (kvt / 10) * 2 + hf, cg = kvt % 10;
        const u32 lb = busy ? STG : wid * 8192;
        u32 sx = 0;
        if (P0A > 0) lds_spin(fr, P0A);
        if (!busy && wid < 4) lds_spin(L_ISS0, 4);
        TS2(1);
        wd_issue(A, wdp, h0, h1, lb, lane);
        if (wid == 6) {
#pragma unroll
            for (int i = 0; i < 10; ++i) e[i] = ld128(g.rx, (stu * 8 + ((lane >> 2) & 3)) * (D * 2) + (lane >> 4) * 64 + (lane & 3) * 16, (i / 5) * 4 * D * 2 + (i % 5) * 256);
        }
        KvRegs R, R2;
        u32x4 w[2];
        { rsrc_t rs = mkrs(A.sk, D * 8); w[0] = ld128(rs, cg * 512 + (lane & 15) * 32, 0); w[1] = ld128(rs, cg * 512 + (lane & 15) * 32, 16); }
        kv_issue(A, R, r32, cg, lane);
        if (nkv == 2) kv_issue(A, R2, r32 + 1, cg, lane);
        SB();
        if (!busy && wid >= 4) lds_inc(L_ISS0);
        PRIO(P0PRIO);
        if (wid == 6) {
            ep += 0x9E3779B1u;
            ldsflagw(L_TAG, ep);
            ldsflagw(L_TAGOK, 1u);
            sx = row_stats(e, lane);
        }
        TS2(2);
        asm volatile("" ::"v"(R.vv[7]), "v"(R2.vv[7]), "v"(sx));
        if (wid == 6) st64(u32x2{sx, ep}, (lane & 14) == (lane >> 4) * 4 ? WS_ST + (stu * 8 + (lane & 1) * 4 + (lane >> 4)) * 8 : OOB, rw, 0);
#pragma unroll 1
        for (int r = 0; r < nkv; ++r) {
            kv_finish(R, w, rw, r32 + r, cg, lane);
            R = R2;
        }
        TS2(3);
        wd_finish(rw, wdp, h0, h1, lb, lane);
        asm volatile("" ::"v"(cpf));
        TS2(4);
        WAITV(0);
        TS2(5);
        lds_spin(L_TAGOK, 1);
        if (lds_add(wid < 4 ? L_PCNT2 : L_PCNT) == (busy ? 1u : 3u) && lane == 0) st32(ldsflag1(L_TAG), WS_FP + (wid < 4 ? 256 + idle : b) * 16, rw);
        if (wid == 7) {
            rsrc_t rh = mkrs(A.hk, D * S * 2);
#pragma unroll
            for (int i = 0; i < 12; ++i) e[i] = ld128n(rh, lane * 16, (i < 8 ? c0 + (i >> 2) : pr * 5 + 2) * 4096 + (i & 3) * 1024);
        }
        PRIO(0);
    }
    const int cgp = (xcd & 1) * 4 + (idx & 3), jr = idx >> 2, blk = ((xcd >> 1) * 8 + jr) * 4;
    const int off = wrapn(13 * (idx & 3) + 4 * (jr & 1) + 16 * (jr >> 1), 54);
    const u32 wsrc = WS_WD + cgp * 5 * 54 * 1024;
    const int w4 = wid & 3;
    bf16x8 wr[6][5];
    if (wid < 4) {
        g1_compute(busy, wr, wsrc, off, wid, lane, rw, rt, grp, T);
        TS(4);
        WAITV(0);
        TS(5);
        const u32 tag = rfl(ldsflag1(L_TAG));
        if (lds_add(L_HCNT) == 3u && lane == 0 && busy) st32(tag, WS_F1 + (rt * 32 + grp) * 128, rw);
        g2_ldw6<99>(wr, rw, wsrc, off, wid, lane, WG0 + 6 * WGS, 30);
        bf16x8 wx[2][5];
        g2_ldw(wx[0], rw, wid == 3 ? OOB : (u32)lane * 16, wsrc, g2_step(off, wid, 6));
        g2_ldw(wx[1], rw, lane * 16, wsrc, g2_step(off, wid, 7));
        SB();
        TS(10);
        lds_spin(fr, 10);
        if (wid == 0) {
            const int r = (blk * 16 + (lane < 32 ? 0 : 63)) / RT;
            g_poll1(rw, WS_F1 + (r * 32 + min(lane & 31, 26)) * 128, tag);
            ldsflagw(L_HRDY, 1u);
        } else lds_spin(L_HRDY, 1);
        TS(6);
        PRIO(0);
        g2_fin(rw, wr, wx, wid, off, blk, lane, T);
        TS(11);
    } else {
        if (wid < 6) { if (busy) { PRIO(2); g1_loader(wid - 4, g, lane, rw); PRIO(0); } }
        TS(1);
        u32x4 v[4];
        if (wid >= 6) {
            const u32 tg = rfl(ldsflag1(L_TAG));
            lds_spin(fr, wid == 7 ? POLL7 : POLLAT);
            if (wid == 7) {
                if (busy) { st_gather(rw, rt * RT, tg, lane); ldsflagw(L_EPI, 2u); }
                lds_spin(L_P0ALL, 1);
            } else {
                const u32 o = WS_FP + lane * 16;
                if (!busy) {
#pragma unroll 1
                    for (int i = 0; i < 64 && (u32)__builtin_amdgcn_s_memrealtime() - ts0 < IDLET; ++i) __builtin_amdgcn_s_sleep(8);
                }
                g_poll4(rw, o, o + 1024, o + 2048, o + 3072, tg);
                ldsflagw(L_P0ALL, 1u);
                if (idle == 0 && lane == 0) st32(tg, 0, rw);
            }
            lds_spin(L_LDONE, 2);
        } else lds_spin(L_P0ALL, 1);
        TS(2);
        const u32 tag = rfl(ldsflag1(L_TAG));
        const u32 dvv = __builtin_amdgcn_raw_buffer_load_b16(mkrs(A.dv, D * 2), (pr * 5 + lane) * 2, 0, 0);
        const u32 so = WS_ST + ((blk + w4) * 16 + (lane & 15)) * 8;
        u32x2 sv = __builtin_amdgcn_raw_buffer_load_b64(rw, so, 0, 16);
        g2_ldw6<THRB>(wr, rw, wsrc, off, wid, lane, 0, WPRE);
        SB();
        TSB(7);
        if (wid >= 6) {
            if (wid == 6) {
                g_poll1(rw, WS_FP + (256 + min(lane, 12)) * 16, tag);
                ldsflagw(L_KVISS, 1u);
#pragma unroll
                for (int i = 0; i < 4; ++i) e[i] = ld128c(rw, WS_KV + lane * 16, (c0 + 1) * 4096 + i * 1024);
            } else lds_spin(L_KVISS, 1);
#pragma unroll
            for (int i = 0; i < 4; ++i) v[i] = ld128c(rw, WS_KV + lane * 16, (wid == 6 ? c0 : pr * 5 + 2) * 4096 + i * 1024);
        }
        SB();
        TSB(8);
        g2_ldw6<99>(wr, rw, wsrc, off, wid, lane, WPRE, WPRE2);
        SB();
        TSB(9);
        if (wid >= 6) {
            lds_spin(fr, 8);
            if (wid == 7) {
#pragma unroll
                for (int i = 0; i < 12; ++i) sts128(IMG + (i >> 2) * IMGSZ + 64 + (i & 3) * 1024 + lane * 16, e[i]);
            } else { const u32x4 e4[4] = {e[0], e[1], e[2], e[3]}; conv_build(e4, IMG + IMGSZ, lane); }
            conv_build(v, IMG + (wid == 6 ? 0 : 2 * IMGSZ), lane);
            lds_inc(L_IMG);
        }
        SB();
        TSB(10);
        g2_ldw6<99>(wr, rw, wsrc, off, wid, lane, WPRE2, 30);
        SB();
        lds_spin(L_IMG, 2);
        TS2(6);
        if (GATE < 7 && busy) {
#pragma unroll 1
            for (int i = 0; i < 0x10000 && rfl(*(volatile const AS3 unsigned char*)(uintptr_t)(L_GD + w4)) == 0; ++i) __builtin_amdgcn_s_sleep(2);
        }
        lds_spin(fr, 10);
        {
            rsrc_t rx = mkrs(A.x, S * D * 2);
#pragma unroll
            for (int j = 0; j < 3; ++j) {
                const int el = j * 64 + lane, r = el / 10;
                dma16(a_area(w4) + (15 + j) * 1024, el < 160 ? ((blk + w4) * 16 + r) * (D * 2) + cgp * 160 + (el - r * 10) * 16 : OOB, rx, 0);
            }
        }
        PRIO(CPRIO);
        TS(3);
        const u32 yst = YST + w4 * 2048;
        const int nt = wid == 7 ? 2 : 1;
        u32 ya = 0;
#pragma unroll 1
        for (int sl = 0; sl < nt; ++sl) {
            const int im = wid == 7 ? sl : w4;
            const int ci = im == 2 ? 2 : mem * 3 + im, cg = (wid == 7 || (mem && w4 == 2)) ? 0 : 1;
            if (sl) y_out(rw, yst, ya, lane * 16, lane);
            ya = WS_Y + (pr * 5 + ci) * 4096 + cg * 2048;
            conv_task(cg, blo(__builtin_amdgcn_readlane(dvv, ci)), IMG + im * IMGSZ, yst, lane, T);
        }
        TS(4);
        *(volatile AS3 unsigned char*)(uintptr_t)(L_BD + w4) = 1;
        const bool late = rfl(ldsflag1(L_HRDY)) != 0;
        const u32 f2 = WS_F2 + (pr * 2 + mem) * F2S;
        if (!late) {
            y_out(rw, yst, ya, lane * 16, lane);
            WAITV(0);
            if (lds_add(L_YCNT) == 3u && lane == 0) st32(tag, f2, rw);
            lds_spin(L_HRDY, 1);
        }
        TS(6);
        PRIO(1);
#pragma unroll 1
        for (int it = 0; it < 0x4000 && __builtin_amdgcn_ballot_w64(sv[1] != tag) != 0; ++it) { asm volatile("" ::: "memory"); sv = __builtin_amdgcn_raw_buffer_load_b64(rw, so, 0, 16); }
        g2_pre(A, rw, wr, w4, off, blk, cgp, tag, sv[0], late ? f2 : 0u, ya, lane, T);
        TS(11);
    }
}

extern "C" void kernel_launch(void* const* d_in, const int* in_sizes, int n_in, void* d_out, int out_size, void* d_ws, size_t ws_size, hipStream_t stream) {
    Args A;
    A.x = (const u16*)d_in[0]; A.k = (const u16*)d_in[1]; A.v = (const u16*)d_in[2]; A.sk = (const u16*)d_in[3]; A.hk = (const u16*)d_in[4];
    A.dv = (const u16*)d_in[5]; A.wg = (const u16*)d_in[7]; A.wu = (const u16*)d_in[8]; A.wd = (const u16*)d_in[9];
    A.out = (u16*)d_out; A.ws = (char*)d_ws;
    (void)hipFuncSetAttribute((const void*)fused_kernel, hipFuncAttributeMaxDynamicSharedMemorySize, LDS_BYTES);
    fused_kernel<<<256, 512, LDS_BYTES, stream>>>(A);
}
```

```cpp
#define EARLYEP
#define OLDORD
#define NOHPF
#include <hip/hip_runtime.h>
#include <stdint.h>
typedef unsigned short u16;
typedef unsigned int u32;
typedef __attribute__((ext_vector_type(8))) __bf16 bf16x8;
typedef __attribute__((ext_vector_type(4))) __bf16 bf16x4;
typedef __attribute__((ext_vector_type(2))) __bf16 bf16x2;
typedef __attribute__((ext_vector_type(4))) float f32x4;
typedef __attribute__((ext_vector_type(2))) float f32x2;
typedef __attribute__((ext_vector_type(4))) unsigned u32x4;
typedef __attribute__((ext_vector_type(2))) unsigned u32x2;
typedef __amdgpu_buffer_rsrc_t rsrc_t;
#define AS3 __attribute__((address_space(3)))
#define DEV __device__ __forceinline__
constexpr int S = 2048, D = 640, DF = 1728, RT = 228;
#define F2S 16
constexpr u32 WS_FP = 4096, WS_F2 = F2S == 16 ? 24576 : 90112, WS_ST = 32768, WS_F1 = 49152;
constexpr u32 WS_KV = 128u << 10, WS_Y = WS_KV + S * D * 2, WS_WD = WS_Y + S * D * 2, WS_H = WS_WD + DF * D * 2, WS_SIZE = 20u << 20;
constexpr u32 XS = 29696, WO = 3 * XS, WSZ = 16384, STG = WO + 4 * WSZ, FLG = STG + 8192, ONES = FLG + 256, CSX = FLG + 512;
constexpr u32 L_FULL = FLG, L_CNT0 = FLG + 8, L_LDONE = FLG + 12, L_FREE = FLG + 16, L_P0ALL = FLG + 32, L_EPI = FLG + 36, L_IMG = FLG + 40,
              L_HCNT = FLG + 52, L_YCNT = FLG + 56, L_HRDY = FLG + 60, L_BD = FLG + 80, L_AD = FLG + 84, L_YRDY = FLG + 88, L_TAG = FLG + 92,
              L_ISS0 = FLG + 64, L_YOK = FLG + 68, L_HM = FLG + 72, L_NIL = FLG + 76, L_AF = FLG + 44, L_CD = FLG + 48, L_CS = FLG + 96, L_AS = FLG + 104, L_TAGOK = FLG + 108, L_PCNT = FLG + 112, L_KVISS = FLG + 116, L_GD = FLG + 120, L_PCNT2 = FLG + 124;
constexpr u32 EPI = STG;
constexpr u32 IMG_U0 = 4160, IMG_U1 = 4160 + 5472, IMGSZ = 4160 + 5472 + 5440;
constexpr u32 IMG = 2 * XS, YST = IMG + 3 * IMGSZ, YT = 40960, RB = 51200;
constexpr u32 LDS_BYTES = 163840;
constexpr u32 OOB = 0x7fffffffu;
#define YN 99
#define SKIP 1
#define YD 5
#define CVALIGN
#define SLI 4
#define SLG "4"
#define BP0 2
#define LKA 8
#define LKB 12
#define YORD 0
#define DEEP 1
#define DEEPA 0
#define GATE 7
#define CPRIO 2
#define WG0 6
#define WGS 1
#define P0S 0
#define YAT 9
#define P0PRIO 3
#define THR 6
#define OUTAUX 0

DEV u32 rfl(u32 v) { return __builtin_amdgcn_readfirstlane(v); }
DEV rsrc_t mkrs(const void* p, u32 bytes) { return __builtin_amdgcn_make_buffer_rsrc((void*)p, 0, bytes, 0x00020000); }
DEV void dma16(u32 m0v, u32 voff, rsrc_t rs, u32 soff) {
    asm volatile("s_mov_b32 m0, %0\n\ts_nop 0\n\tbuffer_load_dwordx4 %1, %2, %3 offen lds" ::"s"(m0v), "v"(voff), "s"(rs), "s"(soff) : "memory");
}
DEV void dma16n(u32 m0v, u32 voff, rsrc_t rs, u32 soff) {
    asm volatile("s_mov_b32 m0, %0\n\ts_nop 0\n\tbuffer_load_dwordx4 %1, %2, %3 offen nt lds" ::"s"(m0v), "v"(voff), "s"(rs), "s"(soff) : "memory");
}
DEV void st128(u32x4 v, u32 voff, rsrc_t rs, u32 soff) {
    asm volatile("buffer_store_dwordx4 %0, %1, %2, %3 offen sc1\n\ts_nop 1" ::"v"(v), "v"(voff), "s"(rs), "s"(soff) : "memory");
}
DEV void st64(u32x2 v, u32 voff, rsrc_t rs, u32 soff) {
    asm volatile("buffer_store_dwordx2 %0, %1, %2, %3 offen sc1\n\ts_nop 1" ::"v"(v), "v"(voff), "s"(rs), "s"(soff) : "memory");
}
DEV void st32(u32 v, u32 voff, rsrc_t rs) { __builtin_amdgcn_raw_buffer_store_b32(v, rs, voff, 0, 16); }
DEV u32x4 ld128(rsrc_t rs, u32 voff, u32 soff) { return __builtin_amdgcn_raw_buffer_load_b128(rs, voff, soff, 0); }
DEV u32x4 ld128n(rsrc_t rs, u32 voff, u32 soff) { return __builtin_amdgcn_raw_buffer_load_b128(rs, voff, soff, 2); }
DEV u32x4 ld128c(rsrc_t rs, u32 voff, u32 soff) { return __builtin_amdgcn_raw_buffer_load_b128(rs, voff, soff, 16); }
DEV u32x2 ld64(rsrc_t rs, u32 voff, u32 soff) { return __builtin_amdgcn_raw_buffer_load_b64(rs, voff, soff, 0); }
DEV bf16x8 lds128(u32 a) { return *(const AS3 bf16x8*)(uintptr_t)a; }
DEV f32x4 lds128f(u32 a) { return *(const AS3 f32x4*)(uintptr_t)a; }
DEV u32x4 lds128u(u32 a) { return *(const AS3 u32x4*)(uintptr_t)a; }
DEV u32 lds32(u32 a) { return *(const AS3 u32*)(uintptr_t)a; }
DEV void sts128(u32 a, u32x4 v) { *(AS3 u32x4*)(uintptr_t)a = v; }
DEV void sts128f(u32 a, f32x4 v) { *(AS3 f32x4*)(uintptr_t)a = v; }
DEV void sts32(u32 a, u32 v) { *(AS3 u32*)(uintptr_t)a = v; }
DEV bf16x4 ldstr(u32 a) { return __builtin_amdgcn_ds_read_tr16_b64_v4bf16((AS3 bf16x4*)(uintptr_t)a); }
DEV u32x2 ldstru(u32 a) { return __builtin_bit_cast(u32x2, ldstr(a)); }
DEV bf16x8 cat(bf16x4 a, bf16x4 b) { return __builtin_shufflevector(a, b, 0, 1, 2, 3, 4, 5, 6, 7); }
DEV bf16x8 asbf(u32x4 v) { return __builtin_bit_cast(bf16x8, v); }
DEV u32 pk2(float a, float b) { f32x2 f = {a, b}; return __builtin_bit_cast(u32, __builtin_convertvector(f, bf16x2)); }
DEV float bfr(float a) { return __uint_as_float(pk2(a, 0.f) << 16); }
DEV float blo(u32 v) { return __uint_as_float(v << 16); }
DEV float bhi(u32 v) { return __uint_as_float(v & 0xffff0000u); }
#define WAITV(n) asm volatile("s_waitcnt vmcnt(" #n ")" ::: "memory")
#define WAITL() asm volatile("s_waitcnt lgkmcnt(0)" ::: "memory")
#define BARL() asm volatile("s_waitcnt lgkmcnt(0)\n\ts_barrier" ::: "memory")
#define PRIO(n) __builtin_amdgcn_s_setprio(n)
#define SB() __builtin_amdgcn_sched_barrier(0)
#define MFMA16(a, b, c) __builtin_amdgcn_mfma_f32_16x16x32_bf16(a, b, c, 0, 0, 0)
DEV u32 ldsflag1(u32 a) { return *(volatile const AS3 u32*)(uintptr_t)a; }
DEV void ldsflagw(u32 a, u32 v) { *(volatile AS3 u32*)(uintptr_t)a = v; }
DEV u32 lds_add(u32 a) {
    u32 r, one = 1;
    asm volatile("s_waitcnt lgkmcnt(0)\n\ts_mov_b64 exec, 1\n\tds_add_rtn_u32 %0, %1, %2\n\ts_mov_b64 exec, -1\n\ts_waitcnt lgkmcnt(0)" : "=&v"(r) : "v"(a), "v"(one) : "memory");
    return rfl(r);
}
DEV void lds_inc(u32 a) {
    u32 one = 1;
    asm volatile("s_mov_b64 exec, 1\n\tds_add_u32 %0, %1\n\ts_mov_b64 exec, -1" ::"v"(a), "v"(one) : "memory");
}
DEV void lds_wait_ge(u32 have, u32 need, u32 vaddr) {
    u32 vt, cn;
    asm volatile(
        "v_cmp_gt_u32 vcc, %3, %2\n\t"
        "s_cbranch_vccnz Lslow%=\n"
        "Ldone%=:\n\t"
        ".subsection 1\n"
        "Lslow%=:\n\t"
        "s_mov_b32 %1, 0x40000\n"
        "Lloop%=:\n\t"
        "ds_read_b32 %0, %4\n\t"
        "s_sub_u32 %1, %1, 1\n\t"
        "s_cbranch_scc1 Ldone%=\n\t"
        "s_waitcnt lgkmcnt(0)\n\t"
        "v_cmp_gt_u32 vcc, %3, %0\n\t"
        "s_cbranch_vccnz Lloop%=\n\t"
        "s_branch Ldone%=\n\t"
        ".subsection 0\n"
        : "=&v"(vt), "=&s"(cn) : "v"(have), "s"(need), "v"(vaddr) : "memory", "scc", "vcc");
}
template <int SL = 1> DEV void lds_spin(u32 vaddr, u32 need) {
    u32 vt, cn;
    asm volatile(
        "s_mov_b32 %1, 0x40000\n"
        "Lsp%=:\n\t"
        "ds_read_b32 %0, %2\n\t"
        "s_waitcnt lgkmcnt(0)\n\t"
        "v_cmp_gt_u32 vcc, %3, %0\n\t"
        "s_cbranch_vccz Lse%=\n\t"
        "s_sleep %4\n\t"
        "s_sub_u32 %1, %1, 1\n\t"
        "s_cbranch_scc0 Lsp%=\n"
        "Lse%=:"
        : "=&v"(vt), "=&s"(cn) : "v"(vaddr), "s"(need), "n"(SL) : "memory", "scc", "vcc");
}
DEV void lds_spinb(u32 vaddr) {
    u32 vt, cn;
    asm volatile(
        "s_mov_b32 %1, 0x20000\n"
        "Lsp%=:\n\t"
        "ds_read_u8 %0, %2\n\t"
        "s_waitcnt lgkmcnt(0)\n\t"
        "v_cmp_eq_u32 vcc, 0, %0\n\t"
        "s_cbranch_vccz Lse%=\n\t"
        "s_sleep 2\n\t"
        "s_sub_u32 %1, %1, 1\n\t"
        "s_cbranch_scc0 Lsp%=\n"
        "Lse%=:"
        : "=&v"(vt), "=&s"(cn) : "v"(vaddr) : "memory", "scc", "vcc");
}
DEV void g_poll1(rsrc_t rs, u32 voff, u32 tag) {
    u32 vt, cn;
    asm volatile(
        "s_mov_b32 %1, 0x8000\n"
        "Lgp%=:\n\t"
        "buffer_load_dword %0, %2, %3, 0 offen sc1\n\t"
        "s_waitcnt vmcnt(0)\n\t"
        "v_cmp_ne_u32 vcc, %4, %0\n\t"
        "s_cbranch_vccz Lge%=\n\t"
        "s_mov_b64 exec, vcc\n\t"
        "s_sleep 4\n\t"
        "s_sub_u32 %1, %1, 1\n\t"
        "s_cbranch_scc0 Lgp%=\n"
        "Lge%=:\n\t"
        "s_mov_b64 exec, -1"
        : "=&v"(vt), "=&s"(cn) : "v"(voff), "s"(rs), "s"(tag) : "memory", "scc", "vcc");
}
DEV void g_pollm(rsrc_t rs, u32 voff, u32 tag, u32 la) {
    u32 vt, cn, m;
    asm volatile(
        "s_mov_b32 %1, 0x8000\n"
        "Lgp%=:\n\t"
        "buffer_load_dword %0, %3, %4, 0 offen sc1\n\t"
        "s_waitcnt vmcnt(0)\n\t"
        "v_cmp_ne_u32 vcc, %5, %0\n\t"
        "s_cbranch_vccz Lge%=\n\t"
        "s_nor_b32 %2, vcc_lo, vcc_hi\n\t"
        "v_mov_b32 %0, %2\n\t"
        "ds_write_b32 %6, %0\n\t"
        "s_mov_b64 exec, vcc\n\t"
        "s_sleep 4\n\t"
        "s_sub_u32 %1, %1, 1\n\t"
        "s_cbranch_scc0 Lgp%=\n"
        "Lge%=:\n\t"
        "s_mov_b64 exec, -1"
        : "=&v"(vt), "=&s"(cn), "=&s"(m) : "v"(voff), "s"(rs), "s"(tag), "v"(la) : "memory", "scc", "vcc");
}
DEV void g_poll2(rsrc_t rs, u32 o0, u32 o1, u32 tag) {
    u32 v0, v1, cn;
    asm volatile(
        "s_mov_b32 %2, 0x4000\n"
        "Lgp%=:\n\t"
        "buffer_load_dword %0, %3, %5, 0 offen sc1\n\t"
        "buffer_load_dword %1, %4, %5, 0 offen sc1\n\t"
        "s_waitcnt vmcnt(0)\n\t"
        "v_xor_b32 %0, %6, %0\n\t"
        "v_xor_b32 %1, %6, %1\n\t"
        "v_or_b32 %0, %0, %1\n\t"
        "v_cmp_ne_u32 vcc, 0, %0\n\t"
        "s_cbranch_vccz Lge%=\n\t"
        "s_mov_b64 exec, vcc\n\t"
        "s_sleep 8\n\t"
        "s_sub_u32 %2, %2, 1\n\t"
        "s_cbranch_scc0 Lgp%=\n"
        "Lge%=:\n\t"
        "s_mov_b64 exec, -1"
        : "=&v"(v0), "=&v"(v1), "=&s"(cn) : "v"(o0), "v"(o1), "s"(rs), "s"(tag) : "memory", "scc", "vcc");
}
DEV void g_poll4(rsrc_t rs, u32 o0, u32 o1, u32 o2, u32 o3, u32 tag) {
    u32 v0, v1, v2, v3, cn;
    asm volatile(
        "s_mov_b32 %4, 0x4000\n"
        "Lgp%=:\n\t"
        "buffer_load_dword %0, %5, %9, 0 offen sc1\n\t"
        "buffer_load_dword %1, %6, %9, 0 offen sc1\n\t"
        "buffer_load_dword %2, %7, %9, 0 offen sc1\n\t"
        "buffer_load_dword %3, %8, %9, 0 offen sc1\n\t"
        "s_waitcnt vmcnt(0)\n\t"
        "v_xor_b32 %0, %10, %0\n\t"
        "v_xor_b32 %1, %10, %1\n\t"
        "v_xor_b32 %2, %10, %2\n\t"
        "v_xor_b32 %3, %10, %3\n\t"
        "v_or3_b32 %0, %0, %1, %2\n\t"
        "v_or_b32 %0, %0, %3\n\t"
        "v_cmp_ne_u32 vcc, 0, %0\n\t"
        "s_cbranch_vccz Lge%=\n\t"
        "s_mov_b64 exec, vcc\n\t"
        "s_sleep 12\n\t"
        "s_sub_u32 %4, %4, 1\n\t"
        "s_cbranch_scc0 Lgp%=\n"
        "Lge%=:\n\t"
        "s_mov_b64 exec, -1"
        : "=&v"(v0), "=&v"(v1), "=&v"(v2), "=&v"(v3), "=&s"(cn) : "v"(o0), "v"(o1), "v"(o2), "v"(o3), "s"(rs), "s"(tag) : "memory", "scc", "vcc");
}

#define TS2(i)
struct TSt {};
#define TS(i)
#define TSB(i)
struct Args { const u16 *x, *k, *v, *sk, *hk, *dv, *wg, *wu, *wd; u16* out; char* ws; };

struct KvRegs { u32x2 kk[11], vv[8]; };
DEV u32x2 ld64n(rsrc_t rs, u32 voff, u32 soff) { return __builtin_amdgcn_raw_buffer_load_b64(rs, voff, soff, 2); }
DEV void kv_issue(const Args& A, KvRegs& R, int r32, int cg, int lane) {
    const int r0 = r32 * 32 + (lane >> 4) * 8;
    rsrc_t rk = mkrs(A.k, S * D * 2), rv = mkrs(A.v, S * D * 2);
    const u32 vo = r32 < 64 ? r0 * (D * 2) + cg * 128 + (lane & 15) * 8 : OOB, vp = (r0 && r32 < 64) ? vo - 3 * D * 2 : OOB;
#pragma unroll
    for (int i = 0; i < 11; ++i) R.kk[i] = i < 3 ? ld64n(rk, vp, i * D * 2) : ld64n(rk, vo, (i - 3) * D * 2);
#pragma unroll
    for (int i = 0; i < 8; ++i) R.vv[i] = ld64n(rv, vo, i * D * 2);
}
DEV void kv_finish(const KvRegs& R, const u32x4 (&w)[2], rsrc_t rw, int r32, int cg, int lane) {
    const u32 so = r32 < 64 ? (lane >> 2) * 4 * 4096 + (2016 - r32 * 32) * 2 + (lane & 3) * 16 : OOB, pl = ((3 - (lane & 3)) * 16 + (lane >> 2)) * 4;
    u32x4 p[4];
#pragma unroll
    for (int c = 0; c < 4; ++c) {
        const u32 wa = w[c >> 1][(c & 1) * 2], wb = w[c >> 1][(c & 1) * 2 + 1];
        const float w0 = blo(wa), w1 = bhi(wa), w2 = blo(wb), w3 = bhi(wb);
        float kf[11], o[8];
#pragma unroll
        for (int i = 0; i < 11; ++i) kf[i] = (c & 1) ? bhi(R.kk[i][c >> 1]) : blo(R.kk[i][c >> 1]);
#pragma unroll
        for (int i = 0; i < 8; ++i) {
            o[i] = __builtin_fmaf(kf[i + 3], w0, __builtin_fmaf(kf[i + 2], w1, __builtin_fmaf(kf[i + 1], w2, kf[i] * w3))) * ((c & 1) ? bhi(R.vv[i][c >> 1]) : blo(R.vv[i][c >> 1]));
            asm("" : "+v"(o[i]));
        }
#pragma unroll
        for (int j = 0; j < 4; ++j) p[c][j] = __builtin_amdgcn_ds_bpermute(pl, pk2(o[7 - 2 * j], o[6 - 2 * j]));
    }
#pragma unroll
    for (int c = 0; c < 4; ++c) st128(p[c], so, rw, WS_KV + (cg * 64 + c) * 4096);
}
DEV void wd_issue(const Args& A, int pu, int h0, int h1, u32 lb, int lane) {
    const int s = pu / 5, l2 = pu - s * 5;
    rsrc_t rd = mkrs(A.wd, DF * D * 2);
    const u32 vo = (32 * s + (lane >> 3)) * (D * 2) + l2 * 256 + (lane & 7) * 16;
#pragma unroll 1
    for (int h = h0; h < h1; ++h)
#pragma unroll
        for (int i = 0; i < 4; ++i) dma16n(lb + h * 4096 + i * 1024, vo, rd, i * 8 * D * 2 + h * 128);
}
DEV void wd_finish(rsrc_t rw, int pu, int h0, int h1, u32 lb, int lane) {
    const int s = pu / 5, l2 = pu - s * 5;
#pragma unroll 1
    for (int h = h0; h < h1; ++h) {
        const u32 ta = lb + h * 4096 + (lane >> 2) * 128 + (lane & 3) * 8;
        u32x2 a[4], b[4];
#pragma unroll
        for (int f = 0; f < 4; ++f) { a[f] = ldstru(ta + f * 32); b[f] = ldstru(ta + f * 32 + 2048); }
#pragma unroll
        for (int f = 0; f < 4; ++f) st128(u32x4{a[f][0], a[f][1], b[f][0], b[f][1]}, lane * 16, rw, WS_WD + ((8 * l2 + 4 * h + f) * 54 + s) * 1024);
    }
}
template <int CTRL> DEV float dppadd(float v) { return v + __uint_as_float(__builtin_amdgcn_update_dpp(0u, __float_as_uint(v), CTRL, 0xf, 0xf, true)); }
DEV u32 row_stats(const u32x4 (&v)[12], int lane) {
    const bf16x8 one = asbf(u32x4{0x3F803F80u, 0x3F803F80u, 0x3F803F80u, 0x3F803F80u});
    float s1[2], s2[2];
#pragma unroll
    for (int a = 0; a < 2; ++a) {
        f32x4 q = {0.f, 0.f, 0.f, 0.f}, s = q;
#pragma unroll
        for (int i = 0; i < 5; ++i) { q = MFMA16(asbf(v[a * 5 + i]), asbf(v[a * 5 + i]), q); s = MFMA16(one, asbf(v[a * 5 + i]), s); }
        const float t = (lane & 2) ? ((lane & 1) ? q[3] : q[2]) : ((lane & 1) ? q[1] : q[0]);
        s2[a] = dppadd<0x4E>(dppadd<0xB1>(t)); s1[a] = dppadd<0x4E>(dppadd<0xB1>(s[0]));
    }
    const float mean = ((lane & 1) ? s1[1] : s1[0]) * (1.f / D);
    const float var = __builtin_fmaxf(__builtin_fmaf(-mean, mean, ((lane & 1) ? s2[1] : s2[0]) * (1.f / D)), 0.f);
    const float den = __builtin_sqrtf(bfr(bfr(var) + 1e-5f));
    return pk2(mean, den);
}
DEV void st_gather(rsrc_t rw, int g0, u32 tag, int lane) {
    u32x4 sv[2];
    const u32 o0 = WS_ST + min(g0 + lane * 2, S - 2) * 8, o1 = WS_ST + min(g0 + 128 + min(lane, 49) * 2, S - 2) * 8;
#pragma unroll 1
    for (int it = 0; it < 0x4000; ++it) {
        asm volatile("" ::: "memory");
        sv[0] = ld128c(rw, o0, 0); sv[1] = ld128c(rw, o1, 0);
        const u32 bad = (sv[0][1] ^ tag) | (sv[0][3] ^ tag) | (sv[1][1] ^ tag) | (sv[1][3] ^ tag);
        if (__builtin_amdgcn_ballot_w64(bad != 0u) == 0) break;
        __builtin_amdgcn_s_sleep(12);
    }
    asm volatile("" ::: "memory");
    const float K1 = -2.f * 1.4426950408889634f * 0.7978845608028654f;
#pragma unroll
    for (int i = 0; i < 2; ++i)
#pragma unroll
        for (int j = 0; j < 2; ++j) {
            const float m = blo(sv[i][2 * j]), den = bhi(sv[i][2 * j]);
            const float r = __builtin_amdgcn_rcpf(den);
            sts128f(EPI + (i * 128 + lane * 2 + j) * 16, f32x4{-m, K1 * r, K1 * 0.044715f * r * r * r, den * den});
        }
}

DEV int wrapn(int v, int n) { return v >= n ? v - n : v; }
DEV int g2_step(int off, int w, int i) { return wrapn(off + (i < 6 ? 8 * i + w : i == 6 ? (w == 2 ? 47 : 52 + (w & 1)) : 48 + w), 54); }
DEV u32 a_area(int w) { return w < 2 ? w * 20480 : 112896 + (w - 2) * 20480; }

template <int TH> DEV void g2_ldw6(bf16x8 (&wr)[6][5], rsrc_t rw, u32 wsrc, int off, int wid, int lane, int j0, int j1) {
#pragma unroll
    for (int j = j0; j < j1; ++j) {
        wr[j / 5][j % 5] = asbf(ld128c(rw, (j >= 25 && wid == 7) ? OOB : (u32)lane * 16, wsrc + ((j % 5) * 54 + g2_step(off, wid, j / 5)) * 1024));
        if (j >= TH && j < 29) asm volatile("s_waitcnt vmcnt(%0)" ::"n"(TH < 63 ? TH : 63) : "memory");
    }
}
DEV void g1_compute(bool busy, bf16x8 (&wr)[6][5], u32 wsrc, int off, int c, int lane, rsrc_t rw, int rt, int grp, TSt& T) {
    const int wi = c >> 1, wj = c & 1;
    const int tok = lane & 15, kq = lane >> 4;
    u32 xa[2], xe[2], wa[2];
    int xm = tok < 8 ? 1 : 0;
    {
        int q = (lane >> 2) & 3, p = lane & 3;
        int f = ((q >> 1) & 1) | ((kq & 1) << 1);
#pragma unroll
        for (int e = 0; e < 2; ++e) {
            wa[e] = WO + 2 * WSZ + (8 * kq + q) * 128 + (((2 * wj + (wi ^ e)) ^ f) << 5) + p * 8;
            u32 t = XS + tok * 128 + (((4 * e + kq) ^ (tok >> 1)) << 4);
            xa[e] = t + wi * 7 * 2048; xe[e] = tok < 8 ? t + 14 * 2048 : ONES;
        }
    }
    f32x4 acc[8][4];
#pragma unroll
    for (int b = 0; b < 8; ++b)
#pragma unroll
        for (int u = 0; u < 4; ++u) acc[b][u] = f32x4{0.f, 0.f, 0.f, 0.f};
    bf16x8 xf[2][8], wf[2][4];
    auto ldf = [&](int ks) {
#pragma unroll
        for (int u = 0; u < 4; ++u) { u32 a = wa[u & 1] + ks * 4096 + (u >> 1) * 8192; wf[ks][u] = cat(ldstr(a), ldstr(a + 512)); }
#pragma unroll
        for (int b = 0; b < 7; ++b) xf[ks][b] = lds128(xa[ks] + b * 2048);
        xf[ks][7] = lds128(xe[ks]);
    };
    auto mm7 = [&](int ks) {
        acc[7][0] = MFMA16(wf[ks][0], xf[ks][7], acc[7][0]);
        acc[7][2] = MFMA16(wf[ks][2], xf[ks][7], acc[7][2]);
    };
    auto mm = [&](int ks, int b0, int b1) {
#pragma unroll
        for (int b = b0; b < b1; ++b)
#pragma unroll
            for (int u = 0; u < 4; ++u) acc[b][u] = MFMA16(wf[ks][u], xf[ks][b], acc[b][u]);
    };
    auto pin = [&]() {
#pragma unroll
        for (int b = 0; b < 7; ++b) asm volatile("" : "+v"(acc[b][0]), "+v"(acc[b][1]), "+v"(acc[b][2]), "+v"(acc[b][3]));
        asm volatile("" : "+v"(acc[7][0]), "+v"(acc[7][2]));
        SB();
    };
    if (busy) {
    lds_spin(L_CNT0, 4);
    TS(1);
    u32 fa = L_FULL + (lane & 1) * 4, fw = L_FREE + c * 4, cnt = 1;
    asm volatile("" : "+v"(fa), "+v"(fw), "+v"(cnt), "+v"(xm));
    int xs = 1, wsl = 2;
    u32 have;
    auto halfA = [&]() {
        ldf(1);
        ldsflagw(fw, cnt);
        have = ldsflag1(fa);
        const int dx = xs == 2 ? -2 * (int)XS : (int)XS, dw = wsl == 3 ? -3 * (int)WSZ : (int)WSZ;
        xs = xs == 2 ? 0 : xs + 1; wsl = (wsl + 1) & 3;
        mm(0, 0, 7); mm7(0);
#pragma unroll
        for (int e = 0; e < 2; ++e) { xa[e] += dx; xe[e] += __mul24(xm, dx); wa[e] += dw; }
        cnt += 1;
#pragma unroll
        for (int i = 0; i < 16; ++i) { __builtin_amdgcn_sched_group_barrier(0x008, 1, 0); __builtin_amdgcn_sched_group_barrier(0x100, 1, 0); }
        __builtin_amdgcn_sched_group_barrier(0x008, 1, 0); __builtin_amdgcn_sched_group_barrier(0x200, 1, 0);
        __builtin_amdgcn_sched_group_barrier(0x008, 1, 0); __builtin_amdgcn_sched_group_barrier(0x100, 1, 0);
#pragma unroll
        for (int i = 0; i < 8; ++i) { __builtin_amdgcn_sched_group_barrier(0x008, 1, 0); __builtin_amdgcn_sched_group_barrier(0x002, 1, 0); }
        __builtin_amdgcn_sched_group_barrier(0x008, 4, 0);
        asm volatile("" : "+v"(xa[0]), "+v"(xa[1]), "+v"(xe[0]), "+v"(xe[1]), "+v"(wa[0]), "+v"(wa[1]), "+v"(cnt), "+v"(have));
        pin();
    };
    ldf(0);
    pin();
    halfA();
#pragma unroll 1
    for (int st = 0; st < 9; ++st) {
        lds_wait_ge(have, st + 1, fa);
        ldf(0);
        mm(1, 0, 7); mm7(1);
#pragma unroll
        for (int i = 0; i < 16; ++i) { __builtin_amdgcn_sched_group_barrier(0x008, 1, 1); __builtin_amdgcn_sched_group_barrier(0x100, 1, 1); }
        __builtin_amdgcn_sched_group_barrier(0x008, 14, 1);
        pin();
        halfA();
    }
    mm7(1); mm(1, 0, 2);
    SB();
    if (tok == 8) {
        sts128f(CSX + (2 * wj + wi) * 64 + kq * 16, acc[7][0]);
        sts128f(CSX + 256 + (2 * wj + wi) * 64 + kq * 16, acc[7][2]);
    }
    lds_inc(L_CS + wj * 4);
    SB();
    mm(1, 2, 7);
    pin();
    TS(2);
    lds_spin(lane & 1 ? L_EPI : L_CS + wj * 4, 2);
    }
    lds_spin(L_P0ALL, 1);
    g2_ldw6<99>(wr, rw, wsrc, off, c, lane, 0, WG0);
    SB();
    TS(3);
    f32x4 cs[4];
#pragma unroll
    for (int u = 0; u < 4; ++u) cs[u] = lds128f(CSX + (u >> 1) * 256 + (2 * wj + (wi ^ (u & 1))) * 64 + kq * 16);
    auto act2 = [&](const f32x4& e, const f32x4& g, const f32x4& u, const f32x4& cg, const f32x4& cu, int i) {
        const f32x2 e0 = {e[0], e[0]}, e1 = {e[1], e[1]}, e2 = {e[2], e[2]}, e3 = {e[3], e[3]};
        const f32x2 a = __builtin_elementwise_fma(e0, f32x2{cg[i], cg[i + 1]}, f32x2{g[i], g[i + 1]});
        const f32x2 b = __builtin_elementwise_fma(e0, f32x2{cu[i], cu[i + 1]}, f32x2{u[i], u[i + 1]});
        const f32x2 z = a * __builtin_elementwise_fma(a * a, e2, e1);
        const f32x2 ex = {__builtin_amdgcn_exp2f(z[0]), __builtin_amdgcn_exp2f(z[1])};
        const f32x2 d = __builtin_elementwise_fma(ex, e3, e3);
        const f32x2 r = (a * b) * f32x2{__builtin_amdgcn_rcpf(d[0]), __builtin_amdgcn_rcpf(d[1])};
        return pk2(r[0], r[1]);
    };
    const int gl = ((rt * RT) & 15) + tok;
    const u32 hv = busy ? (gl >> 4) * (54 * 1024) + (kq * 16 + (gl & 15)) * 16 : OOB;
    const u32 hbase = WS_H + ((((rt * RT) >> 4) + wi * 7) * 54 + grp * 2 + wj) * 1024;
    f32x4 en = lds128f(EPI + (wi * 7 * 16 + tok) * 16);
#pragma unroll
    for (int b = 0; b < 7; ++b) {
        const f32x4 e = en;
        en = lds128f(EPI + ((b < 6 ? wi * 7 + b + 1 : 14) * 16 + tok) * 16);
        SB();
        u32 o[4];
#pragma unroll
        for (int h = 0; h < 2; ++h) { o[2 * h] = act2(e, acc[b][h], acc[b][2 + h], cs[h], cs[2 + h], 0); o[2 * h + 1] = act2(e, acc[b][h], acc[b][2 + h], cs[h], cs[2 + h], 2); }
        u32x4 ov = wi ? u32x4{o[2], o[3], o[0], o[1]} : u32x4{o[0], o[1], o[2], o[3]};
        st128(ov, hv, rw, hbase + b * 54 * 1024);
        if (b < 6) { g2_ldw6<99>(wr, rw, wsrc, off, c, lane, WG0 + b * WGS, WG0 + (b + 1) * WGS); SB(); }
        if (b == GATE) *(volatile AS3 unsigned char*)(uintptr_t)(L_GD + c) = 1;
    }
    if (rt < 8) {
        const f32x4 e = en;
        u32x2 ov = {act2(e, acc[7][0], acc[7][2], cs[0], cs[2], 0), act2(e, acc[7][0], acc[7][2], cs[0], cs[2], 2)};
        st64(ov, tok < 4 ? hv : OOB, rw, hbase + (14 - wi * 7) * 54 * 1024 + wi * 8);
    }
    if (GATE >= 7) *(volatile AS3 unsigned char*)(uintptr_t)(L_GD + c) = 1;
}
struct G1Src { rsrc_t rx, rg, ru; u32 xv0, xv1, xl, wv0, wv1; int ro; };
DEV void g1_loader(int e, const G1Src& g, int lane, rsrc_t rw) {
    const u32 fr = L_FREE + (lane & 3) * 4, fu = L_FULL + e * 4;
    const u32 xv = e ? g.xv1 : g.xv0, wv = e ? g.wv1 : g.wv0;
    auto ld_w = [&](int s) {
        const u32 so = WO + ((s + 2) & 3) * WSZ + e * 1024, ks = wrapn(s + g.ro, 10) * (64 * DF * 2) + e * 8 * DF * 2;
#pragma unroll
        for (int p = 0; p < 16; p += 2) dma16(so + p * 1024, wv, (p >> 3) ? g.ru : g.rg, ks + (p & 7) * 8 * DF * 2);
    };
    ld_w(1);
    int xs = 2;
#pragma unroll 1
    for (int s = 1; s < 10; ++s) {
        if (s > 2 && __builtin_amdgcn_ballot_w64(ldsflag1(fr) < (u32)(s - 2)) != 0) {
            WAITV(8);
            ldsflagw(fu, s - 1);
            lds_spin(fr, s - 2);
        }
        TS2(s);
        const u32 so = xs * XS + e * 1024, ks = wrapn(s + g.ro, 10) * 128 + e * 8 * D * 2;
        xs = xs == 2 ? 0 : xs + 1;
#pragma unroll
        for (int p = 0; p < 28; p += 2) {
            dma16(so + p * 1024, xv, g.rx, ks + p * 8 * D * 2);
            if (p == 14) { WAITV(16); ldsflagw(fu, s - 1); }
        }
        if (!e) dma16(so + 28 * 1024, g.xl, g.rx, ks + 28 * 8 * D * 2);
        if (s < 9) ld_w(s + 1);
    }
    lds_inc(L_LDONE);
    WAITV(0);
    ldsflagw(fu, 9);
    TS2(10);
}

DEV void conv_build(const u32x4 (&v)[4], u32 ib, int lane) {
    const u32 ua = ib + (lane >> 4) * 320 + (lane & 15) * 16;
#pragma unroll
    for (int i = 0; i < 4; ++i) {
        u32 sel = lane == 0 ? (i < 3 ? v[i < 3 ? i + 1 : 0][0] : 0u) : v[i][0];
        u32 nx = __builtin_amdgcn_ds_bpermute(((lane + 1) & 63) * 4, sel);
        sts128(ua + IMG_U0 + i * 1280, v[i]);
        sts128(ua + IMG_U1 + i * 1280, u32x4{__builtin_amdgcn_alignbit(v[i][1], v[i][0], 16), __builtin_amdgcn_alignbit(v[i][2], v[i][1], 16),
                                             __builtin_amdgcn_alignbit(v[i][3], v[i][2], 16), __builtin_amdgcn_alignbit(nx, v[i][3], 16)});
    }
    if (lane < 36) sts128(lane < 32 ? ib + (lane < 16 ? IMG_U0 : IMG_U1) + 5120 + (lane & 15) * 16 : ib + (lane - 32) * 16, u32x4{0u, 0u, 0u, 0u});
}
DEV void conv_task(int g, float dv, u32 ib, u32 yst, u32 gate, int lane, TSt& T) {
    const int a = lane & 15, q = lane >> 4;
    u32 ha = ib + 4 + 4 * a + 16 * q;
    const u32 ub = ib + ((a & 1) ? IMG_U0 : IMG_U1) + 16 * q;
    u32 ua = ub + (15 - 8 * g - (a >> 1)) * 320, un;
    const u32 uz = ub + 5120;
    f32x4 acc[4];
#pragma unroll
    for (int j = 0; j < 8; ++j) {
        const int e = 2046 - 1024 * g - (32 * (j >> 1) + 2 * a + 128 * (2 * q + (j & 1)));
        const u32 uu = lds32(ib + IMG_U0 + (e >> 7) * 320 + (e & 127) * 2);
        acc[j >> 1][2 * (j & 1)] = bhi(uu) * dv; acc[j >> 1][2 * (j & 1) + 1] = blo(uu) * dv;
    }
    u32 cn;
    u32 nit = 4 * g + 3;
    asm volatile(
        "ds_read_b128 v[240:243], %4 offset:0\n\t"
        "ds_read2_b32 v[208:209], %6 offset0:0 offset1:1\n\t"
        "ds_read2_b32 v[210:211], %6 offset0:2 offset1:3\n\t"
        "ds_read_b128 v[244:247], %4 offset:64\n\t"
        "ds_read2_b32 v[212:213], %6 offset0:16 offset1:17\n\t"
        "ds_read2_b32 v[214:215], %6 offset0:18 offset1:19\n\t"
        "ds_read_b128 v[248:251], %4 offset:128\n\t"
        "ds_read2_b32 v[216:217], %6 offset0:32 offset1:33\n\t"
        "ds_read2_b32 v[218:219], %6 offset0:34 offset1:35\n\t"
        "v_mov_b32 v228, 0\n\t"
        "v_mov_b32 v229, 0\n\t"
        "v_mov_b32 v230, 0\n\t"
        "v_mov_b32 v231, 0\n\t"
        "v_mov_b32 v232, 0\n\t"
        "v_mov_b32 v233, 0\n\t"
        "v_mov_b32 v234, 0\n\t"
        "v_mov_b32 v235, 0\n\t"
        "v_mov_b32 v236, 0\n\t"
        "v_mov_b32 v237, 0\n\t"
        "v_mov_b32 v238, 0\n\t"
        "v_mov_b32 v239, 0\n\t"
        "s_mov_b32 %8, 0x20000\n"
        "Lcg%=:\n\t"
        "ds_read_u8 %5, %10\n\t"
        "s_waitcnt lgkmcnt(0)\n\t"
        "v_cmp_eq_u32 vcc, 0, %5\n\t"
        "s_cbranch_vccz Lgo%=\n\t"
        "s_sleep " SLG "\n\t"
        "s_sub_u32 %8, %8, 1\n\t"
        "s_cbranch_scc0 Lcg%=\n"
        "Lgo%=:\n\t"
        "s_setprio 2\n\t"
        CVALIGN
        "Lcv%=:\n\t"
        "s_waitcnt lgkmcnt(6)\n\t"
        "v_mfma_f32_16x16x32_bf16 %0, v[240:243], v[228:231], %0\n\t"
        "ds_read_b128 v[252:255], %4 offset:192\n\t"
        "v_mfma_f32_16x16x32_bf16 %1, v[240:243], v[232:235], %1\n\t"
        "ds_read2_b32 v[220:221], %6 offset0:48 offset1:49\n\t"
        "v_mfma_f32_16x16x32_bf16 %2, v[240:243], v[236:239], %2\n\t"
        "ds_read2_b32 v[222:223], %6 offset0:50 offset1:51\n\t"
        "v_add_u32 %5, 0x140, %4\n\t"
        "v_mfma_f32_16x16x32_bf16 %3, v[240:243], v[208:211], %3\n\t"
        "v_min_u32 %5, %5, %9\n\t"
        "s_waitcnt lgkmcnt(6)\n\t"
        "v_mfma_f32_16x16x32_bf16 %0, v[244:247], v[232:235], %0\n\t"
        "ds_read_b128 v[240:243], %5 offset:0\n\t"
        "v_mfma_f32_16x16x32_bf16 %1, v[244:247], v[236:239], %1\n\t"
        "ds_read2_b32 v[224:225], %6 offset0:64 offset1:65\n\t"
        "v_mfma_f32_16x16x32_bf16 %2, v[244:247], v[208:211], %2\n\t"
        "ds_read2_b32 v[226:227], %6 offset0:66 offset1:67\n\t"
        "v_mfma_f32_16x16x32_bf16 %3, v[244:247], v[212:215], %3\n\t"
        "s_waitcnt lgkmcnt(6)\n\t"
        "v_mfma_f32_16x16x32_bf16 %0, v[248:251], v[236:239], %0\n\t"
        "ds_read_b128 v[244:247], %5 offset:64\n\t"
        "v_mfma_f32_16x16x32_bf16 %1, v[248:251], v[208:211], %1\n\t"
        "ds_read2_b32 v[228:229], %6 offset0:80 offset1:81\n\t"
        "v_mfma_f32_16x16x32_bf16 %2, v[248:251], v[212:215], %2\n\t"
        "ds_read2_b32 v[230:231], %6 offset0:82 offset1:83\n\t"
        "v_add_u32 %4, 0x140, %5\n\t"
        "v_mfma_f32_16x16x32_bf16 %3, v[248:251], v[216:219], %3\n\t"
        "v_min_u32 %4, %4, %9\n\t"
        "s_waitcnt lgkmcnt(6)\n\t"
        "v_mfma_f32_16x16x32_bf16 %0, v[252:255], v[208:211], %0\n\t"
        "ds_read_b128 v[248:251], %5 offset:128\n\t"
        "v_mfma_f32_16x16x32_bf16 %1, v[252:255], v[212:215], %1\n\t"
        "ds_read2_b32 v[232:233], %6 offset0:96 offset1:97\n\t"
        "v_mfma_f32_16x16x32_bf16 %2, v[252:255], v[216:219], %2\n\t"
        "ds_read2_b32 v[234:235], %6 offset0:98 offset1:99\n\t"
        "v_mfma_f32_16x16x32_bf16 %3, v[252:255], v[220:223], %3\n\t"
        "s_waitcnt lgkmcnt(6)\n\t"
        "v_mfma_f32_16x16x32_bf16 %0, v[240:243], v[212:215], %0\n\t"
        "ds_read_b128 v[252:255], %5 offset:192\n\t"
        "v_mfma_f32_16x16x32_bf16 %1, v[240:243], v[216:219], %1\n\t"
        "ds_read2_b32 v[236:237], %6 offset0:112 offset1:113\n\t"
        "v_mfma_f32_16x16x32_bf16 %2, v[240:243], v[220:223], %2\n\t"
        "ds_read2_b32 v[238:239], %6 offset0:114 offset1:115\n\t"
        "v_mfma_f32_16x16x32_bf16 %3, v[240:243], v[224:227], %3\n\t"
        "s_waitcnt lgkmcnt(6)\n\t"
        "v_mfma_f32_16x16x32_bf16 %0, v[244:247], v[216:219], %0\n\t"
        "ds_read_b128 v[240:243], %4 offset:0\n\t"
        "v_mfma_f32_16x16x32_bf16 %1, v[244:247], v[220:223], %1\n\t"
        "ds_read2_b32 v[208:209], %6 offset0:128 offset1:129\n\t"
        "v_mfma_f32_16x16x32_bf16 %2, v[244:247], v[224:227], %2\n\t"
        "ds_read2_b32 v[210:211], %6 offset0:130 offset1:131\n\t"
        "v_mfma_f32_16x16x32_bf16 %3, v[244:247], v[228:231], %3\n\t"
        "s_waitcnt lgkmcnt(6)\n\t"
        "v_mfma_f32_16x16x32_bf16 %0, v[248:251], v[220:223], %0\n\t"
        "ds_read_b128 v[244:247], %4 offset:64\n\t"
        "v_mfma_f32_16x16x32_bf16 %1, v[248:251], v[224:227], %1\n\t"
        "ds_read2_b32 v[212:213], %6 offset0:144 offset1:145\n\t"
        "v_mfma_f32_16x16x32_bf16 %2, v[248:251], v[228:231], %2\n\t"
        "ds_read2_b32 v[214:215], %6 offset0:146 offset1:147\n\t"
        "v_mfma_f32_16x16x32_bf16 %3, v[248:251], v[232:235], %3\n\t"
        "s_sub_u32 %7, %7, 1\n\t"
        "s_waitcnt lgkmcnt(6)\n\t"
        "v_mfma_f32_16x16x32_bf16 %0, v[252:255], v[224:227], %0\n\t"
        "ds_read_b128 v[248:251], %4 offset:128\n\t"
        "v_mfma_f32_16x16x32_bf16 %1, v[252:255], v[228:231], %1\n\t"
        "ds_read2_b32 v[216:217], %6 offset0:160 offset1:161\n\t"
        "v_mfma_f32_16x16x32_bf16 %2, v[252:255], v[232:235], %2\n\t"
        "ds_read2_b32 v[218:219], %6 offset0:162 offset1:163\n\t"
        "v_mfma_f32_16x16x32_bf16 %3, v[252:255], v[236:239], %3\n\t"
        "v_add_u32 %6, 0x200, %6\n\t"
        "s_cbranch_scc0 Lcv%=\n\t"
        "s_waitcnt lgkmcnt(0)\n\t"
        "s_nop 7\n\t"
        "s_nop 7\n\t"
        : "+v"(acc[0]), "+v"(acc[1]), "+v"(acc[2]), "+v"(acc[3]), "+v"(ua), "=&v"(un), "+v"(ha), "+s"(nit), "=&s"(cn)
        : "v"(uz), "v"(gate)
        : "memory", "scc", "vcc", "v208", "v209", "v210", "v211", "v212", "v213", "v214", "v215", "v216", "v217", "v218", "v219", "v220", "v221", "v222", "v223", "v224", "v225", "v226", "v227", "v228", "v229", "v230", "v231", "v232", "v233", "v234", "v235", "v236", "v237", "v238", "v239", "v240", "v241", "v242", "v243", "v244", "v245", "v246", "v247", "v248", "v249", "v250", "v251", "v252", "v253", "v254", "v255");
    TS(10);
#pragma unroll
    for (int j = 0; j < 8; ++j)
        sts32(yst + (32 * ((j >> 1) ^ q) + 2 * a + 128 * (2 * q + (j & 1))) * 2, pk2(acc[j >> 1][2 * (j & 1)], acc[j >> 1][2 * (j & 1) + 1]));
    asm volatile("" ::: "memory");
}
DEV void y_out(rsrc_t rw, u32 yst, u32 ya, u32 vo, int lane) {
    const u32 o = (lane * 16) ^ ((lane >> 5) << 6);
    const u32x4 y0 = lds128u(yst + o), y1 = lds128u(yst + 1024 + (o ^ 128));
    st128(y0, vo, rw, ya);
    st128(y1, vo, rw, ya + 1024);
}

DEV void g2_ldw(bf16x8 (&wr)[5], rsrc_t rw, u32 vo, u32 wsrc, int ks) {
#pragma unroll
    for (int f = 0; f < 5; ++f) wr[f] = asbf(ld128c(rw, vo, wsrc + (f * 54 + ks) * 1024));
}
DEV void mm5_opt(f32x4 (&c)[5], const bf16x8 (&a)[5], bf16x8 h, int w) {
    asm volatile(
        "s_cmp_eq_u32 %11, 3\n\t"
        "s_cbranch_scc1 Lmo%=\n\t"
        "v_mfma_f32_16x16x32_bf16 %0, %5, %10, %0\n\t"
        "v_mfma_f32_16x16x32_bf16 %1, %6, %10, %1\n\t"
        "v_mfma_f32_16x16x32_bf16 %2, %7, %10, %2\n\t"
        "v_mfma_f32_16x16x32_bf16 %3, %8, %10, %3\n\t"
        "v_mfma_f32_16x16x32_bf16 %4, %9, %10, %4\n"
        "Lmo%=:"
        : "+v"(c[0]), "+v"(c[1]), "+v"(c[2]), "+v"(c[3]), "+v"(c[4]) : "v"(a[0]), "v"(a[1]), "v"(a[2]), "v"(a[3]), "v"(a[4]), "v"(h), "s"(w) : "scc");
}
DEV void g2_fin(rsrc_t rw, const bf16x8 (&wr)[6][5], const bf16x8 (&wx)[2][5], int w, int off, int blk, int lane, TSt& T) {
    f32x4 acc[4][5];
#pragma unroll
    for (int t = 0; t < 4; ++t)
#pragma unroll
        for (int f = 0; f < 5; ++f) acc[t][f] = f32x4{0.f, 0.f, 0.f, 0.f};
    bf16x8 hf[32];
    const u32 vz = w == 3 ? OOB : (u32)lane * 16;
    const u32 la = a_area(w) + lane * 16;
    auto ldh = [&](int n) { hf[n] = asbf(ld128c(rw, (n & 7) == 6 ? vz : (u32)lane * 16, WS_H + ((blk + (((n >> 3) + 1 + w) & 3)) * 54 + g2_step(off, w, n & 7)) * 1024)); };
#pragma unroll
    for (int n = 0; n < LKA; ++n) ldh(n);
    SB();
#pragma unroll
    for (int n = 0; n < 32; ++n) {
        const int t = n >> 3, i = n & 7;
        if (SKIP && i == 6) mm5_opt(acc[t], wx[0], hf[n], w);
        else {
#pragma unroll
            for (int f = 0; f < 5; ++f) acc[t][f] = MFMA16(i < 6 ? wr[i < 6 ? i : 0][f] : wx[i & 1][f], hf[n], acc[t][f]);
        }
        if (n + LKA < 32) ldh(n + LKA);
        if (t > 0 && i < 5) sts128f(la + ((t - 1) * 5 + i) * 1024, acc[t > 0 ? t - 1 : 0][i < 5 ? i : 0]);
        if (n == 29) lds_inc(L_AF);
        SB();
    }
    TS(7);
    lds_spin(L_AS, 4);
#pragma unroll
    for (int f = 0; f < 5; ++f) sts128f(la + (15 + f) * 1024, acc[3][f]);
    ldsflagw(L_FREE + w * 4, 11u);
}
DEV void g2_pre(const Args& A, rsrc_t rw, const bf16x8 (&wr)[6][5], int w, int off, int blk, int cgp, u32 tag, u32 sv, u32 f2, u32 ya, int lane, TSt& T) {
    f32x4 acc[4][5];
#pragma unroll
    for (int t = 0; t < 4; ++t)
#pragma unroll
        for (int f = 0; f < 5; ++f) acc[t][f] = f32x4{0.f, 0.f, 0.f, 0.f};
    bf16x8 hf[24];
    u32x4 yv[3];
    u32x2 xv[5];
    const u32 yfa = WS_F2 + (cgp * 32 + (lane & 31)) * F2S, yo = (lane >> 3) * 4096 + (lane & 7) * 16;
    auto ldy = [&](u32 vo) {
#pragma unroll
        for (int j = 0; j < 3; ++j) yv[j] = ld128c(rw, (j == 2 && w >= 2) ? OOB : vo, WS_Y + (cgp * 80 + (j * 4 + w) * 8) * 4096 + blk * 32);
    };
    const u32 vz = w == 3 ? OOB : (u32)lane * 16;
    auto ldh = [&](int n) { hf[n] = asbf(ld128c(rw, n % 6 == 5 ? vz : (u32)lane * 16, WS_H + ((blk + ((n / 6 + 1 + w) & 3)) * 54 + g2_step(off, w + 4, n % 6)) * 1024)); };
    y_out(rw, YST + w * 2048, ya, f2 ? (u32)lane * 16 : OOB, lane);
    lds_inc(L_CD);
    SB();
#pragma unroll
    for (int n = 0; n < LKB; ++n) ldh(n);
    SB();
    asm volatile("s_waitcnt vmcnt(%0)" ::"n"(LKB) : "memory");
    st32(tag, (lds_add(f2 ? L_YCNT : L_NIL) == 3u && lane == 0 && f2) ? f2 : OOB, rw);
#pragma unroll
    for (int f = 0; f < 5; ++f) xv[f] = *(const AS3 u32x2*)(uintptr_t)(a_area(w) + 15 * 1024 + (lane & 15) * 160 + (lane >> 4) * 8 + f * 32);
    lds_inc(L_AS);
    TS(5);
    SB();
#pragma unroll
    for (int n = 0; n < 24; ++n) {
        const int t = n / 6, i = n % 6;
        if (n == 6) lds_spin(L_CD, 4);
        if (SKIP && i == 5) mm5_opt(acc[t], wr[5], hf[n], w);
        else {
#pragma unroll
            for (int f = 0; f < 5; ++f) acc[t][f] = MFMA16(wr[i][f], hf[n], acc[t][f]);
        }
        if (n + LKB < 24) ldh(n + LKB);
        if (t > 0 && i < 5) sts128f(RB + ((((t + w) & 3) * 3 + t - 1) * 5 + i) * 1024 + lane * 16, acc[t > 0 ? t - 1 : 0][i < 5 ? i : 0]);
        SB();
    }
    asm volatile("s_nop 7\n\ts_nop 7" ::: "memory");
    lds_inc(L_AD);
    TS(7);
    if (w == 2) { g_poll1(rw, yfa, tag); ldsflagw(L_YOK, 1u); }
    else lds_spin(L_YOK, 1);
    ldy(yo);
    TS(8);
    SB();
    f32x4 xn[5], pr[15];
    const u32 ta = RB + w * 15360;
    lds_spin(L_AD, 4);
#pragma unroll
    for (int j = 0; j < 15; ++j) pr[j] = lds128f(ta + j * 1024 + lane * 16);
    {
        const float r = __builtin_amdgcn_rcpf(bhi(sv)), mr = -blo(sv) * r;
#pragma unroll
        for (int f = 0; f < 5; ++f) {
            const f32x4 xx = {blo(xv[f][0]), bhi(xv[f][0]), blo(xv[f][1]), bhi(xv[f][1])};
            acc[3][f] += xx;
            xn[f] = xx * r + mr;
        }
    }
#pragma unroll
    for (int f = 0; f < 5; ++f) acc[3][f] = (acc[3][f] + pr[f]) + (pr[5 + f] + pr[10 + f]);
    SB();
    lds_spin(L_AF, 4);
#pragma unroll
    for (int j = 0; j < 15; ++j) pr[j] = lds128f(a_area((w + 3 - j / 5) & 3) + j * 1024 + lane * 16);
#pragma unroll
    for (int f = 0; f < 5; ++f) acc[3][f] = (acc[3][f] + pr[f]) + (pr[5 + f] + pr[10 + f]);
    SB();
#pragma unroll
    for (int j = 0; j < 3; ++j) if (j < 2 || w < 2) sts128(YT + (j * 4 + w) * 1024 + lane * 16, yv[j]);
    lds_inc(L_YRDY);
    lds_spin(L_YRDY, 4);
#pragma unroll
    for (int f = 0; f < 5; ++f) {
        const u32x2 y = ldstru(YT + (16 * f + (lane >> 2)) * 128 + w * 32 + (lane & 3) * 8);
        acc[3][f] += xn[f] * f32x4{blo(y[0]), bhi(y[0]), blo(y[1]), bhi(y[1])};
    }
    SB();
    lds_spin(L_FREE + w * 4, 11);
    TS(9);
#pragma unroll
    for (int f = 0; f < 5; ++f) pr[f] = lds128f(a_area(w) + (15 + f) * 1024 + lane * 16);
#pragma unroll
    for (int f = 0; f < 5; ++f) {
        const f32x4 o = acc[3][f] + pr[f];
        *(AS3 u32x2*)(uintptr_t)(ta + (lane & 15) * 160 + (lane >> 4) * 8 + f * 32) = u32x2{pk2(o[0], o[1]), pk2(o[2], o[3])};
    }
    asm volatile("" ::: "memory");
    rsrc_t ro = mkrs(A.out, S * D * 2);
#pragma unroll
    for (int j = 0; j < 3; ++j) {
        const int e = j * 64 + lane, r = e / 10;
        __builtin_amdgcn_raw_buffer_store_b128(lds128u(ta + e * 16), ro, e < 160 ? ((blk + w) * 16 + r) * (D * 2) + cgp * 160 + (e - r * 10) * 16 : OOB, 0, OUTAUX);
    }
}

#define P0A 0
#define WPRE 15
#define WPRE2 30
#define IDLET 880u
#define THRB 99
#define POLL7 6
#define POLLAT 6
__global__ __launch_bounds__(512, 2) void fused_kernel(Args A) {
    const int b = blockIdx.x, lane = threadIdx.x & 63, wid = rfl(threadIdx.x >> 6);
    const int xcd = b & 7, idx = b >> 3;
    rsrc_t rw = mkrs(A.ws, WS_SIZE);
    TSt T = {};
    TS(0);
    const int hx = (xcd + 2) >> 3, n4 = xcd * 4, dv = (0x45333333u >> n4) & 15, c = ((0x540u >> xcd) & 32) + idx;
    const int cq = (c * ((0x344056u >> (dv * 8 - 24)) & 255)) >> 8, cr = c - cq * dv, over = c - dv * (21 - 15 * hx);
    const int rt = cr + ((0x50663300u >> n4) & 15), grp = cq + 21 * hx;
    const int idle = (over + ((0x53221100u >> n4) & 15)) | (over >> 31);
    const int tr = cq * (1 + hx) + cr * (3 - hx) + ((0xB0221100u >> n4) & 15);
    int ro = tr - 10 * ((tr * 26) >> 8);
    const bool busy = idle < 0;
    if (threadIdx.x >= 448 && lane < 36) ldsflagw(lane < 32 ? FLG + lane * 4 : ONES + (lane - 32) * 4, lane >= 32 ? 0x3F803F80u : (!busy && lane < 8) ? 10u : 0u);
    G1Src g;
    g.rx = mkrs(A.x, S * D * 2); g.rg = mkrs(A.wg, D * DF * 2); g.ru = mkrs(A.wu, D * DF * 2); g.ro = ro;
    { int row = lane >> 3;
      g.xv0 = (rt * RT + row) * (D * 2) + (((lane & 7) ^ (row >> 1)) << 4); g.xv1 = g.xv0 ^ 64; g.xl = row < 4 ? g.xv0 : OOB;
      g.wv0 = row * (DF * 2) + grp * 128 + (((lane & 7) ^ (((row >> 1) & 1) << 1)) << 4); g.wv1 = g.wv0 ^ 64; }
    if (wid < 4) {
        PRIO(3);
        if (busy) {
            if (wid & 1) { g.xv0 = g.xv1; g.wv0 = g.wv1; g.xl = OOB; }
            const u32 ks = ro * 128 + (wid & 1) * 8 * D * 2, kw = ro * (64 * DF * 2) + (wid & 1) * 8 * DF * 2, lo = (wid & 1) * 1024;
            if (wid < 2) {
#pragma unroll
                for (int p = 0; p < 22; p += 2) dma16(XS + lo + p * 1024, g.xv0, g.rx, ks + p * 8 * D * 2);
            } else {
#pragma unroll
                for (int p = 0; p < 16; p += 2) dma16(WO + 2 * WSZ + lo + p * 1024, g.wv0, (p >> 3) ? g.ru : g.rg, kw + (p & 7) * 8 * DF * 2);
#pragma unroll
                for (int p = 22; p < 28; p += 2) dma16(XS + lo + p * 1024, g.xv0, g.rx, ks + p * 8 * D * 2);
                if (wid == 2) dma16(XS + 28 * 1024, g.xl, g.rx, ks + 28 * 8 * D * 2);
            }
        }
    }
    u32 cpf = 0, ep = 0;
        if (wid == 7) {
            uint64_t pc; u32 len;
            asm volatile("s_getpc_b64 %0\nLcs%=:\n\ts_mov_b32 %1, Lce%=-Lcs%=\n\t.subsection 2\nLce%=:\n\t.subsection 0" : "=s"(pc), "=s"(len));
            cpf = __builtin_amdgcn_raw_buffer_load_b32(mkrs((const void*)pc, min(len, 40960u)), lane < 10 ? (idx * 10 + lane) * 128 : OOB, 0, 0);
        }
        if (wid == 6) ep = __builtin_amdgcn_raw_buffer_load_b32(rw, 0, 0, 16);
    BARL();
    const u32 ts0 = (u32)__builtin_amdgcn_s_memrealtime();
    if (busy && wid < 4) { PRIO(1); WAITV(0);
        lds_inc(L_CNT0); }
    const int pr = (b >> 4) * 8 + xcd, mem = idx & 1;
    const int c0 = pr * 5 + mem * 3;
    u32x4 e[12];
    const u32 fr = L_FREE + (lane & 3) * 4;
    if (!busy || wid >= 6) {
        const int q = rt * 27 + grp;
        int kvt = 320, hf = 0, nkv = 1, wdp = -1, stu = -1;
        int h0 = 0, h1 = 0;
        if (busy) { kvt = q; hf = wid - 6; wdp = q; h0 = 1 - hf; h1 = 2 - hf; stu = q + q / 18; }
        else {
            if (wid < 4) { kvt = 243 + idle * 6 + wid; nkv = 2; }
            else { kvt = 245 + idle * 6 + (wid >> 1); hf = wid & 1; wdp = 241 + 2 * idle + (wid >> 1); h0 = hf; h1 = hf + 1; }
            stu = 19 * idle + 18;
            if (kvt == 320) { nkv = 0; wdp = 268 + hf; h0 = 0; h1 = 2; }
        }
        const int r32 = (kvt / 10) * 2 + hf, cg = kvt % 10;
        const u32 lb = busy ? STG : wid * 8192;
        u32 sx = 0;
        if (P0A > 0) lds_spin(fr, P0A);
        if (!busy && wid < 4) lds_spin(L_ISS0, 4);
        TS2(1);
        wd_issue(A, wdp, h0, h1, lb, lane);
        if (wid == 6) {
#pragma unroll
            for (int i = 0; i < 10; ++i) e[i] = ld128(g.rx, (stu * 8 + ((lane >> 2) & 3)) * (D * 2) + (lane >> 4) * 64 + (lane & 3) * 16, (i / 5) * 4 * D * 2 + (i % 5) * 256);
        }
        KvRegs R, R2;
        u32x4 w[2];
        { rsrc_t rs = mkrs(A.sk, D * 8); w[0] = ld128(rs, cg * 512 + (lane & 15) * 32, 0); w[1] = ld128(rs, cg * 512 + (lane & 15) * 32, 16); }
        kv_issue(A, R, r32, cg, lane);
        if (nkv == 2) kv_issue(A, R2, r32 + 1, cg, lane);
        SB();
        if (!busy && wid >= 4) lds_inc(L_ISS0);
        PRIO(P0PRIO);
        if (wid == 6) {
            ep += 0x9E3779B1u;
            ldsflagw(L_TAG, ep);
            ldsflagw(L_TAGOK, 1u);
            sx = row_stats(e, lane);
        }
        TS2(2);
        asm volatile("" ::"v"(R.vv[7]), "v"(R2.vv[7]), "v"(sx));
        if (wid == 6) st64(u32x2{sx, ep}, (lane & 14) == (lane >> 4) * 4 ? WS_ST + (stu * 8 + (lane & 1) * 4 + (lane >> 4)) * 8 : OOB, rw, 0);
#pragma unroll 1
        for (int r = 0; r < nkv; ++r) {
            kv_finish(R, w, rw, r32 + r, cg, lane);
            R = R2;
        }
        TS2(3);
        wd_finish(rw, wdp, h0, h1, lb, lane);
        asm volatile("" ::"v"(cpf));
        TS2(4);
        WAITV(0);
        TS2(5);
        lds_spin(L_TAGOK, 1);
        if (lds_add(wid < 4 ? L_PCNT2 : L_PCNT) == (busy ? 1u : 3u) && lane == 0) st32(ldsflag1(L_TAG), WS_FP + (wid < 4 ? 256 + idle : b) * 16, rw);
        if (wid == 7) {
            rsrc_t rh = mkrs(A.hk, D * S * 2);
#pragma unroll
            for (int i = 0; i < 12; ++i) e[i] = ld128n(rh, lane * 16, (i < 8 ? c0 + (i >> 2) : pr * 5 + 2) * 4096 + (i & 3) * 1024);
        }
        PRIO(0);
    }
    const int cgp = (xcd & 1) * 4 + (idx & 3), jr = idx >> 2, blk = ((xcd >> 1) * 8 + jr) * 4;
    const int off = wrapn(13 * (idx & 3) + 4 * (jr & 1) + 16 * (jr >> 1), 54);
    const u32 wsrc = WS_WD + cgp * 5 * 54 * 1024;
    const int w4 = wid & 3;
    bf16x8 wr[6][5];
    if (wid < 4) {
        g1_compute(busy, wr, wsrc, off, wid, lane, rw, rt, grp, T);
        TS(4);
        WAITV(0);
        TS(5);
        const u32 tag = rfl(ldsflag1(L_TAG));
        if (lds_add(L_HCNT) == 3u && lane == 0 && busy) st32(tag, WS_F1 + (rt * 32 + grp) * 128, rw);
        g2_ldw6<99>(wr, rw, wsrc, off, wid, lane, WG0 + 6 * WGS, 30);
        bf16x8 wx[2][5];
        g2_ldw(wx[0], rw, wid == 3 ? OOB : (u32)lane * 16, wsrc, g2_step(off, wid, 6));
        g2_ldw(wx[1], rw, lane * 16, wsrc, g2_step(off, wid, 7));
        SB();
        TS(10);
        lds_spin(fr, 10);
        if (wid == 0) {
            const int r = (blk * 16 + (lane < 32 ? 0 : 63)) / RT;
            g_poll1(rw, WS_F1 + (r * 32 + min(lane & 31, 26)) * 128, tag);
            ldsflagw(L_HRDY, 1u);
        } else lds_spin(L_HRDY, 1);
        TS(6);
        PRIO(0);
        g2_fin(rw, wr, wx, wid, off, blk, lane, T);
        TS(11);
    } else {
        if (wid < 6) { if (busy) { PRIO(2); g1_loader(wid - 4, g, lane, rw); PRIO(0); } }
        TS(1);
        u32x4 v[4];
        if (wid >= 6) {
            const u32 tg = rfl(ldsflag1(L_TAG));
            lds_spin(fr, wid == 7 ? POLL7 : POLLAT);
            if (wid == 7) {
                if (busy) { st_gather(rw, rt * RT, tg, lane); ldsflagw(L_EPI, 2u); }
                lds_spin(L_P0ALL, 1);
            } else {
                const u32 o = WS_FP + lane * 16;
                if (!busy) {
#pragma unroll 1
                    for (int i = 0; i < 64 && (u32)__builtin_amdgcn_s_memrealtime() - ts0 < IDLET; ++i) __builtin_amdgcn_s_sleep(8);
                }
                g_poll4(rw, o, o + 1024, o + 2048, o + 3072, tg);
                ldsflagw(L_P0ALL, 1u);
                if (idle == 0 && lane == 0) st32(tg, 0, rw);
            }
            lds_spin(L_LDONE, 2);
        } else lds_spin(L_P0ALL, 1);
        TS(2);
        PRIO(BP0);
        const u32 tag = rfl(ldsflag1(L_TAG));
        const u32 dvv = __builtin_amdgcn_raw_buffer_load_b16(mkrs(A.dv, D * 2), (pr * 5 + lane) * 2, 0, 0);
        const u32 so = WS_ST + ((blk + w4) * 16 + (lane & 15)) * 8;
        u32x2 sv = __builtin_amdgcn_raw_buffer_load_b64(rw, so, 0, 16);
        g2_ldw6<THRB>(wr, rw, wsrc, off, wid, lane, 0, WPRE);
        SB();
        TSB(7);
        if (wid >= 6) {
            if (wid == 6) {
                g_poll1(rw, WS_FP + (256 + min(lane, 12)) * 16, tag);
                ldsflagw(L_KVISS, 1u);
#pragma unroll
                for (int i = 0; i < 4; ++i) e[i] = ld128c(rw, WS_KV + lane * 16, (c0 + 1) * 4096 + i * 1024);
            } else lds_spin(L_KVISS, 1);
#pragma unroll
            for (int i = 0; i < 4; ++i) v[i] = ld128c(rw, WS_KV + lane * 16, (wid == 6 ? c0 : pr * 5 + 2) * 4096 + i * 1024);
        }
        SB();
        TSB(8);
        g2_ldw6<99>(wr, rw, wsrc, off, wid, lane, WPRE, WPRE2);
        SB();
        TSB(9);
        if (wid >= 6) {
            lds_spin(fr, 8);
            if (wid == 7) {
#pragma unroll
                for (int i = 0; i < 12; ++i) sts128(IMG + (i >> 2) * IMGSZ + 64 + (i & 3) * 1024 + lane * 16, e[i]);
            } else { const u32x4 e4[4] = {e[0], e[1], e[2], e[3]}; conv_build(e4, IMG + IMGSZ, lane); }
            conv_build(v, IMG + (wid == 6 ? 0 : 2 * IMGSZ), lane);
            lds_inc(L_IMG);
        }
        SB();
        TSB(10);
        g2_ldw6<99>(wr, rw, wsrc, off, wid, lane, WPRE2, 30);
        SB();
        lds_spin<SLI>(L_IMG, 2);
        TS2(6);
        lds_spin(fr, 10);
        {
            rsrc_t rx = mkrs(A.x, S * D * 2);
#pragma unroll
            for (int j = 0; j < 3; ++j) {
                const int el = j * 64 + lane, r = el / 10;
                dma16(a_area(w4) + (15 + j) * 1024, el < 160 ? ((blk + w4) * 16 + r) * (D * 2) + cgp * 160 + (el - r * 10) * 16 : OOB, rx, 0);
            }
        }
        TS(3);
        const u32 yst = YST + w4 * 2048;
        const int nt = wid == 7 ? 2 : 1;
        u32 ya = 0;
#pragma unroll 1
        for (int sl = 0; sl < nt; ++sl) {
            const int im = wid == 7 ? sl : w4;
            const int ci = im == 2 ? 2 : mem * 3 + im, cg = (wid == 7 || (mem && w4 == 2)) ? 0 : 1;
            if (sl) y_out(rw, yst, ya, lane * 16, lane);
            ya = WS_Y + (pr * 5 + ci) * 4096 + cg * 2048;
            conv_task(cg, blo(__builtin_amdgcn_readlane(dvv, ci)), IMG + im * IMGSZ, yst, L_GD + w4, lane, T);
        }
        TS(4);
        *(volatile AS3 unsigned char*)(uintptr_t)(L_BD + w4) = 1;
        const bool late = rfl(ldsflag1(L_HRDY)) != 0;
        const u32 f2 = WS_F2 + (pr * 2 + mem) * F2S;
        if (!late) {
            y_out(rw, yst, ya, lane * 16, lane);
            WAITV(0);
            if (lds_add(L_YCNT) == 3u && lane == 0) st32(tag, f2, rw);
            lds_spin(L_HRDY, 1);
        }
        TS(6);
        PRIO(1);
#pragma unroll 1
        for (int it = 0; it < 0x4000 && __builtin_amdgcn_ballot_w64(sv[1] != tag) != 0; ++it) { asm volatile("" ::: "memory"); sv = __builtin_amdgcn_raw_buffer_load_b64(rw, so, 0, 16); }
        g2_pre(A, rw, wr, w4, off, blk, cgp, tag, sv[0], late ? f2 : 0u, ya, lane, T);
        TS(11);
    }
}

extern "C" void kernel_launch(void* const* d_in, const int* in_sizes, int n_in, void* d_out, int out_size, void* d_ws, size_t ws_size, hipStream_t stream) {
    Args A;
    A.x = (const u16*)d_in[0]; A.k = (const u16*)d_in[1]; A.v = (const u16*)d_in[2]; A.sk = (const u16*)d_in[3]; A.hk = (const u16*)d_in[4];
    A.dv = (const u16*)d_in[5]; A.wg = (const u16*)d_in[7]; A.wu = (const u16*)d_in[8]; A.wd = (const u16*)d_in[9];
    A.out = (u16*)d_out; A.ws = (char*)d_ws;
    (void)hipFuncSetAttribute((const void*)fused_kernel, hipFuncAttributeMaxDynamicSharedMemorySize, LDS_BYTES);
    fused_kernel<<<256, 512, LDS_BYTES, stream>>>(A);
}
```

```cpp
#define GPRIO 2
#define BPL 1
#define BPM 1
#include <hip/hip_runtime.h>
#include <stdint.h>
typedef unsigned short u16;
typedef unsigned int u32;
typedef __attribute__((ext_vector_type(8))) __bf16 bf16x8;
typedef __attribute__((ext_vector_type(4))) __bf16 bf16x4;
typedef __attribute__((ext_vector_type(2))) __bf16 bf16x2;
typedef __attribute__((ext_vector_type(4))) float f32x4;
typedef __attribute__((ext_vector_type(2))) float f32x2;
typedef __attribute__((ext_vector_type(4))) unsigned u32x4;
typedef __attribute__((ext_vector_type(2))) unsigned u32x2;
typedef __amdgpu_buffer_rsrc_t rsrc_t;
#define AS3 __attribute__((address_space(3)))
#define DEV __device__ __forceinline__
constexpr int S = 2048, D = 640, DF = 1728, RT = 228;
#define F2S 16
constexpr u32 WS_FP = 4096, WS_F2 = F2S == 16 ? 24576 : 90112, WS_ST = 32768, WS_F1 = 49152;
constexpr u32 WS_KV = 128u << 10, WS_Y = WS_KV + S * D * 2, WS_WD = WS_Y + S * D * 2, WS_H = WS_WD + DF * D * 2, WS_SIZE = 20u << 20;
constexpr u32 XS = 29696, WO = 3 * XS, WSZ = 16384, STG = WO + 4 * WSZ, FLG = STG + 8192, ONES = FLG + 256, CSX = FLG + 512;
constexpr u32 L_FULL = FLG, L_CNT0 = FLG + 8, L_LDONE = FLG + 12, L_FREE = FLG + 16, L_P0ALL = FLG + 32, L_EPI = FLG + 36, L_IMG = FLG + 40,
              L_HCNT = FLG + 52, L_YCNT = FLG + 56, L_HRDY = FLG + 60, L_BD = FLG + 80, L_AD = FLG + 84, L_YRDY = FLG + 88, L_TAG = FLG + 92,
              L_ISS0 = FLG + 64, L_YOK = FLG + 68, L_HM = FLG + 72, L_NIL = FLG + 76, L_AF = FLG + 44, L_CD = FLG + 48, L_CS = FLG + 96, L_AS = FLG + 104, L_TAGOK = FLG + 108, L_PCNT = FLG + 112, L_KVISS = FLG + 116, L_GD = FLG + 120, L_PCNT2 = FLG + 124;
constexpr u32 EPI = STG;
constexpr u32 IMG_U0 = 4160, IMG_U1 = 4160 + 5472, IMGSZ = 4160 + 5472 + 5440;
constexpr u32 IMG = 2 * XS, YST = IMG + 3 * IMGSZ, YT = 40960, RB = 51200;
constexpr u32 LDS_BYTES = 163840;
constexpr u32 OOB = 0x7fffffffu;
#define SKIP 1
#define CVALIGN
#define SLW "s_sleep 1\n\t"
#define SLL 8
#define SLH 2
#define SLP 1
#define PW 3
#define CPA "2"
#define SLI 4
#define SLG "4"
#define LKA 8
#define LKB 12
#define GATE 7
#define WG0 0
#define WGS 2
#define P0PRIO 3
#define THR 6
#define OUTAUX 0

DEV u32 rfl(u32 v) { return __builtin_amdgcn_readfirstlane(v); }
DEV rsrc_t mkrs(const void* p, u32 bytes) { return __builtin_amdgcn_make_buffer_rsrc((void*)p, 0, bytes, 0x00020000); }
DEV void dma16(u32 m0v, u32 voff, rsrc_t rs, u32 soff) {
    asm volatile("s_mov_b32 m0, %0\n\ts_nop 0\n\tbuffer_load_dwordx4 %1, %2, %3 offen lds" ::"s"(m0v), "v"(voff), "s"(rs), "s"(soff) : "memory");
}
DEV void dma16n(u32 m0v, u32 voff, rsrc_t rs, u32 soff) {
    asm volatile("s_mov_b32 m0, %0\n\ts_nop 0\n\tbuffer_load_dwordx4 %1, %2, %3 offen nt lds" ::"s"(m0v), "v"(voff), "s"(rs), "s"(soff) : "memory");
}
DEV void st128(u32x4 v, u32 voff, rsrc_t rs, u32 soff) {
    asm volatile("buffer_store_dwordx4 %0, %1, %2, %3 offen sc1\n\ts_nop 1" ::"v"(v), "v"(voff), "s"(rs), "s"(soff) : "memory");
}
DEV void st64(u32x2 v, u32 voff, rsrc_t rs, u32 soff) {
    asm volatile("buffer_store_dwordx2 %0, %1, %2, %3 offen sc1\n\ts_nop 1" ::"v"(v), "v"(voff), "s"(rs), "s"(soff) : "memory");
}
DEV void st32(u32 v, u32 voff, rsrc_t rs) { __builtin_amdgcn_raw_buffer_store_b32(v, rs, voff, 0, 16); }
DEV u32x4 ld128(rsrc_t rs, u32 voff, u32 soff) { return __builtin_amdgcn_raw_buffer_load_b128(rs, voff, soff, 0); }
DEV u32x4 ld128n(rsrc_t rs, u32 voff, u32 soff) { return __builtin_amdgcn_raw_buffer_load_b128(rs, voff, soff, 2); }
DEV u32x4 ld128c(rsrc_t rs, u32 voff, u32 soff) { return __builtin_amdgcn_raw_buffer_load_b128(rs, voff, soff, 16); }
DEV bf16x8 lds128(u32 a) { return *(const AS3 bf16x8*)(uintptr_t)a; }
DEV f32x4 lds128f(u32 a) { return *(const AS3 f32x4*)(uintptr_t)a; }
DEV u32x4 lds128u(u32 a) { return *(const AS3 u32x4*)(uintptr_t)a; }
DEV u32 lds32(u32 a) { return *(const AS3 u32*)(uintptr_t)a; }
DEV void sts128(u32 a, u32x4 v) { *(AS3 u32x4*)(uintptr_t)a = v; }
DEV void sts128f(u32 a, f32x4 v) { *(AS3 f32x4*)(uintptr_t)a = v; }
DEV void sts32(u32 a, u32 v) { *(AS3 u32*)(uintptr_t)a = v; }
DEV bf16x4 ldstr(u32 a) { return __builtin_amdgcn_ds_read_tr16_b64_v4bf16((AS3 bf16x4*)(uintptr_t)a); }
DEV u32x2 ldstru(u32 a) { return __builtin_bit_cast(u32x2, ldstr(a)); }
DEV bf16x8 cat(bf16x4 a, bf16x4 b) { return __builtin_shufflevector(a, b, 0, 1, 2, 3, 4, 5, 6, 7); }
DEV bf16x8 asbf(u32x4 v) { return __builtin_bit_cast(bf16x8, v); }
DEV u32 pk2(float a, float b) { f32x2 f = {a, b}; return __builtin_bit_cast(u32, __builtin_convertvector(f, bf16x2)); }
DEV float bfr(float a) { return __uint_as_float(pk2(a, 0.f) << 16); }
DEV float blo(u32 v) { return __uint_as_float(v << 16); }
DEV float bhi(u32 v) { return __uint_as_float(v & 0xffff0000u); }
#define WAITV(n) asm volatile("s_waitcnt vmcnt(" #n ")" ::: "memory")
#define BARL() asm volatile("s_waitcnt lgkmcnt(0)\n\ts_barrier" ::: "memory")
#define PRIO(n) __builtin_amdgcn_s_setprio(n)
#define SB() __builtin_amdgcn_sched_barrier(0)
#define MFMA16(a, b, c) __builtin_amdgcn_mfma_f32_16x16x32_bf16(a, b, c, 0, 0, 0)
DEV u32 ldsflag1(u32 a) { return *(volatile const AS3 u32*)(uintptr_t)a; }
DEV void ldsflagw(u32 a, u32 v) { *(volatile AS3 u32*)(uintptr_t)a = v; }
DEV u32 lds_add(u32 a) {
    u32 r, one = 1;
    asm volatile("s_waitcnt lgkmcnt(0)\n\ts_mov_b64 exec, 1\n\tds_add_rtn_u32 %0, %1, %2\n\ts_mov_b64 exec, -1\n\ts_waitcnt lgkmcnt(0)" : "=&v"(r) : "v"(a), "v"(one) : "memory");
    return rfl(r);
}
DEV void lds_inc(u32 a) {
    u32 one = 1;
    asm volatile("s_mov_b64 exec, 1\n\tds_add_u32 %0, %1\n\ts_mov_b64 exec, -1" ::"v"(a), "v"(one) : "memory");
}
DEV void lds_wait_ge(u32 have, u32 need, u32 vaddr) {
    u32 vt, cn;
    asm volatile(
        "v_cmp_gt_u32 vcc, %3, %2\n\t"
        "s_cbranch_vccnz Lslow%=\n"
        "Ldone%=:\n\t"
        ".subsection 1\n"
        "Lslow%=:\n\t"
        "s_mov_b32 %1, 0x40000\n"
        "Lloop%=:\n\t"
        SLW
        "ds_read_b32 %0, %4\n\t"
        "s_sub_u32 %1, %1, 1\n\t"
        "s_cbranch_scc1 Ldone%=\n\t"
        "s_waitcnt lgkmcnt(0)\n\t"
        "v_cmp_gt_u32 vcc, %3, %0\n\t"
        "s_cbranch_vccnz Lloop%=\n\t"
        "s_branch Ldone%=\n\t"
        ".subsection 0\n"
        : "=&v"(vt), "=&s"(cn) : "v"(have), "s"(need), "v"(vaddr) : "memory", "scc", "vcc");
}
template <int SL = 1> DEV void lds_spin(u32 vaddr, u32 need) {
    u32 vt, cn;
    asm volatile(
        "s_mov_b32 %1, 0x40000\n"
        "Lsp%=:\n\t"
        "ds_read_b32 %0, %2\n\t"
        "s_waitcnt lgkmcnt(0)\n\t"
        "v_cmp_gt_u32 vcc, %3, %0\n\t"
        "s_cbranch_vccz Lse%=\n\t"
        "s_sleep %4\n\t"
        "s_sub_u32 %1, %1, 1\n\t"
        "s_cbranch_scc0 Lsp%=\n"
        "Lse%=:"
        : "=&v"(vt), "=&s"(cn) : "v"(vaddr), "s"(need), "n"(SL) : "memory", "scc", "vcc");
}
DEV void g_poll1(rsrc_t rs, u32 voff, u32 tag) {
    u32 vt, cn;
    asm volatile(
        "s_mov_b32 %1, 0x8000\n"
        "Lgp%=:\n\t"
        "buffer_load_dword %0, %2, %3, 0 offen sc1\n\t"
        "s_waitcnt vmcnt(0)\n\t"
        "v_cmp_ne_u32 vcc, %4, %0\n\t"
        "s_cbranch_vccz Lge%=\n\t"
        "s_mov_b64 exec, vcc\n\t"
        "s_sleep 4\n\t"
        "s_sub_u32 %1, %1, 1\n\t"
        "s_cbranch_scc0 Lgp%=\n"
        "Lge%=:\n\t"
        "s_mov_b64 exec, -1"
        : "=&v"(vt), "=&s"(cn) : "v"(voff), "s"(rs), "s"(tag) : "memory", "scc", "vcc");
}
DEV void g_poll4(rsrc_t rs, u32 o0, u32 o1, u32 o2, u32 o3, u32 tag) {
    u32 v0, v1, v2, v3, cn;
    asm volatile(
        "s_mov_b32 %4, 0x4000\n"
        "Lgp%=:\n\t"
        "buffer_load_dword %0, %5, %9, 0 offen sc1\n\t"
        "buffer_load_dword %1, %6, %9, 0 offen sc1\n\t"
        "buffer_load_dword %2, %7, %9, 0 offen sc1\n\t"
        "buffer_load_dword %3, %8, %9, 0 offen sc1\n\t"
        "s_waitcnt vmcnt(0)\n\t"
        "v_xor_b32 %0, %10, %0\n\t"
        "v_xor_b32 %1, %10, %1\n\t"
        "v_xor_b32 %2, %10, %2\n\t"
        "v_xor_b32 %3, %10, %3\n\t"
        "v_or3_b32 %0, %0, %1, %2\n\t"
        "v_or_b32 %0, %0, %3\n\t"
        "v_cmp_ne_u32 vcc, 0, %0\n\t"
        "s_cbranch_vccz Lge%=\n\t"
        "s_mov_b64 exec, vcc\n\t"
        "s_sleep 12\n\t"
        "s_sub_u32 %4, %4, 1\n\t"
        "s_cbranch_scc0 Lgp%=\n"
        "Lge%=:\n\t"
        "s_mov_b64 exec, -1"
        : "=&v"(v0), "=&v"(v1), "=&v"(v2), "=&v"(v3), "=&s"(cn) : "v"(o0), "v"(o1), "v"(o2), "v"(o3), "s"(rs), "s"(tag) : "memory", "scc", "vcc");
}

struct Args { const u16 *x, *k, *v, *sk, *hk, *dv, *wg, *wu, *wd; u16* out; char* ws; };

struct KvRegs { u32x2 kk[11], vv[8]; };
DEV u32x2 ld64n(rsrc_t rs, u32 voff, u32 soff) { return __builtin_amdgcn_raw_buffer_load_b64(rs, voff, soff, 2); }
DEV void kv_issue(const Args& A, KvRegs& R, int r32, int cg, int lane) {
    const int r0 = r32 * 32 + (lane >> 4) * 8;
    rsrc_t rk = mkrs(A.k, S * D * 2), rv = mkrs(A.v, S * D * 2);
    const u32 vo = r32 < 64 ? r0 * (D * 2) + cg * 128 + (lane & 15) * 8 : OOB, vp = (r0 && r32 < 64) ? vo - 3 * D * 2 : OOB;
#pragma unroll
    for (int i = 0; i < 11; ++i) R.kk[i] = i < 3 ? ld64n(rk, vp, i * D * 2) : ld64n(rk, vo, (i - 3) * D * 2);
#pragma unroll
    for (int i = 0; i < 8; ++i) R.vv[i] = ld64n(rv, vo, i * D * 2);
}
DEV void kv_finish(const KvRegs& R, const u32x4 (&w)[2], rsrc_t rw, int r32, int cg, int lane) {
    const u32 so = r32 < 64 ? (lane >> 2) * 4 * 4096 + (2016 - r32 * 32) * 2 + (lane & 3) * 16 : OOB, pl = ((3 - (lane & 3)) * 16 + (lane >> 2)) * 4;
    u32x4 p[4];
#pragma unroll
    for (int c = 0; c < 4; ++c) {
        const u32 wa = w[c >> 1][(c & 1) * 2], wb = w[c >> 1][(c & 1) * 2 + 1];
        const float w0 = blo(wa), w1 = bhi(wa), w2 = blo(wb), w3 = bhi(wb);
        float kf[11], o[8];
#pragma unroll
        for (int i = 0; i < 11; ++i) kf[i] = (c & 1) ? bhi(R.kk[i][c >> 1]) : blo(R.kk[i][c >> 1]);
#pragma unroll
        for (int i = 0; i < 8; ++i) {
            o[i] = __builtin_fmaf(kf[i + 3], w0, __builtin_fmaf(kf[i + 2], w1, __builtin_fmaf(kf[i + 1], w2, kf[i] * w3))) * ((c & 1) ? bhi(R.vv[i][c >> 1]) : blo(R.vv[i][c >> 1]));
            asm("" : "+v"(o[i]));
        }
#pragma unroll
        for (int j = 0; j < 4; ++j) p[c][j] = __builtin_amdgcn_ds_bpermute(pl, pk2(o[7 - 2 * j], o[6 - 2 * j]));
    }
#pragma unroll
    for (int c = 0; c < 4; ++c) st128(p[c], so, rw, WS_KV + (cg * 64 + c) * 4096);
}
DEV void wd_issue(const Args& A, int pu, int h0, int h1, u32 lb, int lane) {
    const int s = pu / 5, l2 = pu - s * 5;
    rsrc_t rd = mkrs(A.wd, DF * D * 2);
    const u32 vo = (32 * s + (lane >> 3)) * (D * 2) + l2 * 256 + (lane & 7) * 16;
#pragma unroll 1
    for (int h = h0; h < h1; ++h)
#pragma unroll
        for (int i = 0; i < 4; ++i) dma16n(lb + h * 4096 + i * 1024, vo, rd, i * 8 * D * 2 + h * 128);
}
DEV void wd_finish(rsrc_t rw, int pu, int h0, int h1, u32 lb, int lane) {
    const int s = pu / 5, l2 = pu - s * 5;
#pragma unroll 1
    for (int h = h0; h < h1; ++h) {
        const u32 ta = lb + h * 4096 + (lane >> 2) * 128 + (lane & 3) * 8;
        u32x2 a[4], b[4];
#pragma unroll
        for (int f = 0; f < 4; ++f) { a[f] = ldstru(ta + f * 32); b[f] = ldstru(ta + f * 32 + 2048); }
#pragma unroll
        for (int f = 0; f < 4; ++f) st128(u32x4{a[f][0], a[f][1], b[f][0], b[f][1]}, lane * 16, rw, WS_WD + ((8 * l2 + 4 * h + f) * 54 + s) * 1024);
    }
}
template <int CTRL> DEV float dppadd(float v) { return v + __uint_as_float(__builtin_amdgcn_update_dpp(0u, __float_as_uint(v), CTRL, 0xf, 0xf, true)); }
DEV u32 row_stats(const u32x4 (&v)[12], int lane) {
    const bf16x8 one = asbf(u32x4{0x3F803F80u, 0x3F803F80u, 0x3F803F80u, 0x3F803F80u});
    float s1[2], s2[2];
#pragma unroll
    for (int a = 0; a < 2; ++a) {
        f32x4 q = {0.f, 0.f, 0.f, 0.f}, s = q;
#pragma unroll
        for (int i = 0; i < 5; ++i) { q = MFMA16(asbf(v[a * 5 + i]), asbf(v[a * 5 + i]), q); s = MFMA16(one, asbf(v[a * 5 + i]), s); }
        const float t = (lane & 2) ? ((lane & 1) ? q[3] : q[2]) : ((lane & 1) ? q[1] : q[0]);
        s2[a] = dppadd<0x4E>(dppadd<0xB1>(t)); s1[a] = dppadd<0x4E>(dppadd<0xB1>(s[0]));
    }
    const float mean = ((lane & 1) ? s1[1] : s1[0]) * (1.f / D);
    const float var = __builtin_fmaxf(__builtin_fmaf(-mean, mean, ((lane & 1) ? s2[1] : s2[0]) * (1.f / D)), 0.f);
    const float den = __builtin_sqrtf(bfr(bfr(var) + 1e-5f));
    return pk2(mean, den);
}
DEV void st_gather(rsrc_t rw, int g0, u32 tag, int lane) {
    u32x4 sv[2];
    const u32 o0 = WS_ST + min(g0 + lane * 2, S - 2) * 8, o1 = WS_ST + min(g0 + 128 + min(lane, 49) * 2, S - 2) * 8;
#pragma unroll 1
    for (int it = 0; it < 0x4000; ++it) {
        asm volatile("" ::: "memory");
        sv[0] = ld128c(rw, o0, 0); sv[1] = ld128c(rw, o1, 0);
        const u32 bad = (sv[0][1] ^ tag) | (sv[0][3] ^ tag) | (sv[1][1] ^ tag) | (sv[1][3] ^ tag);
        if (__builtin_amdgcn_ballot_w64(bad != 0u) == 0) break;
        __builtin_amdgcn_s_sleep(12);
    }
    asm volatile("" ::: "memory");
    const float K1 = -2.f * 1.4426950408889634f * 0.7978845608028654f;
#pragma unroll
    for (int i = 0; i < 2; ++i)
#pragma unroll
        for (int j = 0; j < 2; ++j) {
            const float m = blo(sv[i][2 * j]), den = bhi(sv[i][2 * j]);
            const float r = __builtin_amdgcn_rcpf(den);
            sts128f(EPI + (i * 128 + lane * 2 + j) * 16, f32x4{-m, K1 * r, K1 * 0.044715f * r * r * r, den * den});
        }
}

DEV int wrapn(int v, int n) { return v >= n ? v - n : v; }
DEV int g2_step(int off, int w, int i) { return wrapn(off + (i < 6 ? 8 * i + w : i == 6 ? (w == 2 ? 47 : 52 + (w & 1)) : 48 + w), 54); }
DEV u32 a_area(int w) { return w < 2 ? w * 20480 : 112896 + (w - 2) * 20480; }

template <int TH> DEV void g2_ldw6(bf16x8 (&wr)[6][5], rsrc_t rw, u32 wsrc, int off, int wid, int lane, int j0, int j1) {
#pragma unroll
    for (int j = j0; j < j1; ++j) {
        wr[j / 5][j % 5] = asbf(ld128c(rw, (j >= 25 && wid == 7) ? OOB : (u32)lane * 16, wsrc + ((j % 5) * 54 + g2_step(off, wid, j / 5)) * 1024));
        if (j >= TH && j < 29) asm volatile("s_waitcnt vmcnt(%0)" ::"n"(TH < 63 ? TH : 63) : "memory");
    }
}
DEV void g1_compute(bool busy, bf16x8 (&wr)[6][5], u32 wsrc, int off, int c, int lane, rsrc_t rw, int rt, int grp) {
    const int wi = c >> 1, wj = c & 1;
    const int tok = lane & 15, kq = lane >> 4;
    u32 xa[2], xe[2], wa[2];
    int xm = tok < 8 ? 1 : 0;
    {
        int q = (lane >> 2) & 3, p = lane & 3;
        int f = ((q >> 1) & 1) | ((kq & 1) << 1);
#pragma unroll
        for (int e = 0; e < 2; ++e) {
            wa[e] = WO + 2 * WSZ + (8 * kq + q) * 128 + (((2 * wj + (wi ^ e)) ^ f) << 5) + p * 8;
            u32 t = XS + tok * 128 + (((4 * e + kq) ^ (tok >> 1)) << 4);
            xa[e] = t + wi * 7 * 2048; xe[e] = tok < 8 ? t + 14 * 2048 : ONES;
        }
    }
    f32x4 acc[8][4];
#pragma unroll
    for (int b = 0; b < 8; ++b)
#pragma unroll
        for (int u = 0; u < 4; ++u) acc[b][u] = f32x4{0.f, 0.f, 0.f, 0.f};
    bf16x8 xf[2][8], wf[2][4];
    auto ldf = [&](int ks) {
#pragma unroll
        for (int u = 0; u < 4; ++u) { u32 a = wa[u & 1] + ks * 4096 + (u >> 1) * 8192; wf[ks][u] = cat(ldstr(a), ldstr(a + 512)); }
#pragma unroll
        for (int b = 0; b < 7; ++b) xf[ks][b] = lds128(xa[ks] + b * 2048);
        xf[ks][7] = lds128(xe[ks]);
    };
    auto mm7 = [&](int ks) {
        acc[7][0] = MFMA16(wf[ks][0], xf[ks][7], acc[7][0]);
        acc[7][2] = MFMA16(wf[ks][2], xf[ks][7], acc[7][2]);
    };
    auto mm = [&](int ks, int b0, int b1) {
#pragma unroll
        for (int b = b0; b < b1; ++b)
#pragma unroll
            for (int u = 0; u < 4; ++u) acc[b][u] = MFMA16(wf[ks][u], xf[ks][b], acc[b][u]);
    };
    auto pin = [&]() {
#pragma unroll
        for (int b = 0; b < 7; ++b) asm volatile("" : "+v"(acc[b][0]), "+v"(acc[b][1]), "+v"(acc[b][2]), "+v"(acc[b][3]));
        asm volatile("" : "+v"(acc[7][0]), "+v"(acc[7][2]));
        SB();
    };
    f32x4 cs[4], en;
    auto rdc = [&]() {
#pragma unroll
        for (int u = 0; u < 4; ++u) cs[u] = lds128f(CSX + (u >> 1) * 256 + (2 * wj + (wi ^ (u & 1))) * 64 + kq * 16);
        en = lds128f(EPI + (wi * 7 * 16 + tok) * 16);
    };
    if (busy) {
    lds_spin<SLH>(L_CNT0, 4);
    u32 fa = L_FULL + (lane & 1) * 4, fw = L_FREE + c * 4, cnt = 1;
    asm volatile("" : "+v"(fa), "+v"(fw), "+v"(cnt), "+v"(xm));
    int xs = 1, wsl = 2;
    u32 have;
    auto halfA = [&]() {
        ldf(1);
        ldsflagw(fw, cnt);
        have = ldsflag1(fa);
        const int dx = xs == 2 ? -2 * (int)XS : (int)XS, dw = wsl == 3 ? -3 * (int)WSZ : (int)WSZ;
        xs = xs == 2 ? 0 : xs + 1; wsl = (wsl + 1) & 3;
        mm(0, 0, 7); mm7(0);
#pragma unroll
        for (int e = 0; e < 2; ++e) { xa[e] += dx; xe[e] += __mul24(xm, dx); wa[e] += dw; }
        cnt += 1;
#pragma unroll
        for (int i = 0; i < 16; ++i) { __builtin_amdgcn_sched_group_barrier(0x008, 1, 0); __builtin_amdgcn_sched_group_barrier(0x100, 1, 0); }
        __builtin_amdgcn_sched_group_barrier(0x008, 1, 0); __builtin_amdgcn_sched_group_barrier(0x200, 1, 0);
        __builtin_amdgcn_sched_group_barrier(0x008, 1, 0); __builtin_amdgcn_sched_group_barrier(0x100, 1, 0);
#pragma unroll
        for (int i = 0; i < 8; ++i) { __builtin_amdgcn_sched_group_barrier(0x008, 1, 0); __builtin_amdgcn_sched_group_barrier(0x002, 1, 0); }
        __builtin_amdgcn_sched_group_barrier(0x008, 4, 0);
        asm volatile("" : "+v"(xa[0]), "+v"(xa[1]), "+v"(xe[0]), "+v"(xe[1]), "+v"(wa[0]), "+v"(wa[1]), "+v"(cnt), "+v"(have));
        pin();
    };
    ldf(0);
    pin();
    halfA();
#pragma unroll 1
    for (int st = 0; st < 9; ++st) {
        lds_wait_ge(have, st + 1, fa);
        ldf(0);
        mm(1, 0, 7); mm7(1);
#pragma unroll
        for (int i = 0; i < 16; ++i) { __builtin_amdgcn_sched_group_barrier(0x008, 1, 1); __builtin_amdgcn_sched_group_barrier(0x100, 1, 1); }
        __builtin_amdgcn_sched_group_barrier(0x008, 14, 1);
        pin();
        halfA();
    }
    mm7(1); mm(1, 0, 2);
    SB();
    if (tok == 8) {
        sts128f(CSX + (2 * wj + wi) * 64 + kq * 16, acc[7][0]);
        sts128f(CSX + 256 + (2 * wj + wi) * 64 + kq * 16, acc[7][2]);
    }
    lds_inc(L_CS + wj * 4);
    SB();
    mm(1, 2, 7);
    pin();
    lds_spin(lane & 2 ? L_P0ALL : lane & 1 ? L_EPI : L_CS + wj * 4, 2);
    } else lds_spin<SLP>(L_P0ALL, 1);
    rdc();
    g2_ldw6<99>(wr, rw, wsrc, off, c, lane, 0, WG0);
    SB();
    PRIO(GPRIO);
    auto act2 = [&](const f32x4& e, const f32x4& g, const f32x4& u, const f32x4& cg, const f32x4& cu, int i) {
        const f32x2 e0 = {e[0], e[0]}, e1 = {e[1], e[1]}, e2 = {e[2], e[2]}, e3 = {e[3], e[3]};
        const f32x2 a = __builtin_elementwise_fma(e0, f32x2{cg[i], cg[i + 1]}, f32x2{g[i], g[i + 1]});
        const f32x2 b = __builtin_elementwise_fma(e0, f32x2{cu[i], cu[i + 1]}, f32x2{u[i], u[i + 1]});
        const f32x2 z = a * __builtin_elementwise_fma(a * a, e2, e1);
        const f32x2 ex = {__builtin_amdgcn_exp2f(z[0]), __builtin_amdgcn_exp2f(z[1])};
        const f32x2 d = __builtin_elementwise_fma(ex, e3, e3);
        const f32x2 r = (a * b) * f32x2{__builtin_amdgcn_rcpf(d[0]), __builtin_amdgcn_rcpf(d[1])};
        return pk2(r[0], r[1]);
    };
    const int gl = ((rt * RT) & 15) + tok;
    const u32 hv = busy ? (gl >> 4) * (54 * 1024) + (kq * 16 + (gl & 15)) * 16 : OOB;
    const u32 hbase = WS_H + ((((rt * RT) >> 4) + wi * 7) * 54 + grp * 2 + wj) * 1024;
#pragma unroll
    for (int b = 0; b < 7; ++b) {
        const f32x4 e = en;
        en = lds128f(EPI + ((b < 6 ? wi * 7 + b + 1 : 14) * 16 + tok) * 16);
        SB();
        u32 o[4];
#pragma unroll
        for (int h = 0; h < 2; ++h) { o[2 * h] = act2(e, acc[b][h], acc[b][2 + h], cs[h], cs[2 + h], 0); o[2 * h + 1] = act2(e, acc[b][h], acc[b][2 + h], cs[h], cs[2 + h], 2); }
        u32x4 ov = wi ? u32x4{o[2], o[3], o[0], o[1]} : u32x4{o[0], o[1], o[2], o[3]};
        st128(ov, hv, rw, hbase + b * 54 * 1024);
        if (b < 6) { g2_ldw6<99>(wr, rw, wsrc, off, c, lane, WG0 + b * WGS, WG0 + (b + 1) * WGS); SB(); }
        if (b == GATE) *(volatile AS3 unsigned char*)(uintptr_t)(L_GD + c) = 1;
    }
    if (rt < 8) {
        const f32x4 e = en;
        u32x2 ov = {act2(e, acc[7][0], acc[7][2], cs[0], cs[2], 0), act2(e, acc[7][0], acc[7][2], cs[0], cs[2], 2)};
        st64(ov, tok < 4 ? hv : OOB, rw, hbase + (14 - wi * 7) * 54 * 1024 + wi * 8);
    }
    if (GATE >= 7) *(volatile AS3 unsigned char*)(uintptr_t)(L_GD + c) = 1;
}
struct G1Src { rsrc_t rx, rg, ru; u32 xv0, xv1, xl, wv0, wv1; int ro; };
DEV void g1_loader(int e, const G1Src& g, int lane, rsrc_t rw) {
    const u32 fr = L_FREE + (lane & 3) * 4, fu = L_FULL + e * 4;
    const u32 xv = e ? g.xv1 : g.xv0, wv = e ? g.wv1 : g.wv0;
    auto ld_w = [&](int s) {
        const u32 so = WO + ((s + 2) & 3) * WSZ + e * 1024, ks = wrapn(s + g.ro, 10) * (64 * DF * 2) + e * 8 * DF * 2;
#pragma unroll
        for (int p = 0; p < 16; p += 2) dma16(so + p * 1024, wv, (p >> 3) ? g.ru : g.rg, ks + (p & 7) * 8 * DF * 2);
    };
    ld_w(1);
    int xs = 2;
#pragma unroll 1
    for (int s = 1; s < 10; ++s) {
        if (s > 2 && __builtin_amdgcn_ballot_w64(ldsflag1(fr) < (u32)(s - 2)) != 0) {
            WAITV(8);
            ldsflagw(fu, s - 1);
            lds_spin<SLH>(fr, s - 2);
        }
        const u32 so = xs * XS + e * 1024, ks = wrapn(s + g.ro, 10) * 128 + e * 8 * D * 2;
        xs = xs == 2 ? 0 : xs + 1;
#pragma unroll
        for (int p = 0; p < 28; p += 2) {
            dma16(so + p * 1024, xv, g.rx, ks + p * 8 * D * 2);
            if (p == 14) { WAITV(16); ldsflagw(fu, s - 1); }
        }
        if (!e) dma16(so + 28 * 1024, g.xl, g.rx, ks + 28 * 8 * D * 2);
        if (s < 9) ld_w(s + 1);
    }
    lds_inc(L_LDONE);
    WAITV(0);
    ldsflagw(fu, 9);
}

DEV void conv_build(const u32x4 (&v)[4], u32 ib, int lane) {
    const u32 ua = ib + (lane >> 4) * 320 + (lane & 15) * 16;
#pragma unroll
    for (int i = 0; i < 4; ++i) {
        u32 sel = lane == 0 ? (i < 3 ? v[i < 3 ? i + 1 : 0][0] : 0u) : v[i][0];
        u32 nx = __builtin_amdgcn_ds_bpermute(((lane + 1) & 63) * 4, sel);
        sts128(ua + IMG_U0 + i * 1280, v[i]);
        sts128(ua + IMG_U1 + i * 1280, u32x4{__builtin_amdgcn_alignbit(v[i][1], v[i][0], 16), __builtin_amdgcn_alignbit(v[i][2], v[i][1], 16),
                                             __builtin_amdgcn_alignbit(v[i][3], v[i][2], 16), __builtin_amdgcn_alignbit(nx, v[i][3], 16)});
    }
    if (lane < 36) sts128(lane < 32 ? ib + (lane < 16 ? IMG_U0 : IMG_U1) + 5120 + (lane & 15) * 16 : ib + (lane - 32) * 16, u32x4{0u, 0u, 0u, 0u});
}
DEV void conv_task(int g, float dv, u32 ib, u32 yst, u32 gate, int lane) {
    const int a = lane & 15, q = lane >> 4;
    u32 ha = ib + 4 + 4 * a + 16 * q;
    const u32 ub = ib + ((a & 1) ? IMG_U0 : IMG_U1) + 16 * q;
    u32 ua = ub + (15 - 8 * g - (a >> 1)) * 320, un;
    const u32 uz = ub + 5120;
    f32x4 acc[4];
#pragma unroll
    for (int j = 0; j < 8; ++j) {
        const int e = 2046 - 1024 * g - (32 * (j >> 1) + 2 * a + 128 * (2 * q + (j & 1)));
        const u32 uu = lds32(ib + IMG_U0 + (e >> 7) * 320 + (e & 127) * 2);
        acc[j >> 1][2 * (j & 1)] = bhi(uu) * dv; acc[j >> 1][2 * (j & 1) + 1] = blo(uu) * dv;
    }
    u32 cn;
    u32 nit = 4 * g + 3;
    asm volatile(
        "ds_read_b128 v[240:243], %4 offset:0\n\t"
        "ds_read2_b32 v[208:209], %6 offset0:0 offset1:1\n\t"
        "ds_read2_b32 v[210:211], %6 offset0:2 offset1:3\n\t"
        "ds_read_b128 v[244:247], %4 offset:64\n\t"
        "ds_read2_b32 v[212:213], %6 offset0:16 offset1:17\n\t"
        "ds_read2_b32 v[214:215], %6 offset0:18 offset1:19\n\t"
        "ds_read_b128 v[248:251], %4 offset:128\n\t"
        "ds_read2_b32 v[216:217], %6 offset0:32 offset1:33\n\t"
        "ds_read2_b32 v[218:219], %6 offset0:34 offset1:35\n\t"
        "v_mov_b32 v228, 0\n\t"
        "v_mov_b32 v229, 0\n\t"
        "v_mov_b32 v230, 0\n\t"
        "v_mov_b32 v231, 0\n\t"
        "v_mov_b32 v232, 0\n\t"
        "v_mov_b32 v233, 0\n\t"
        "v_mov_b32 v234, 0\n\t"
        "v_mov_b32 v235, 0\n\t"
        "v_mov_b32 v236, 0\n\t"
        "v_mov_b32 v237, 0\n\t"
        "v_mov_b32 v238, 0\n\t"
        "v_mov_b32 v239, 0\n\t"
        "s_mov_b32 %8, 0x20000\n"
        "Lcg%=:\n\t"
        "ds_read_u8 %5, %10\n\t"
        "s_waitcnt lgkmcnt(0)\n\t"
        "v_cmp_eq_u32 vcc, 0, %5\n\t"
        "s_cbranch_vccz Lgo%=\n\t"
        "s_sleep " SLG "\n\t"
        "s_sub_u32 %8, %8, 1\n\t"
        "s_cbranch_scc0 Lcg%=\n"
        "Lgo%=:\n\t"
        "s_setprio " CPA "\n\t"
        CVALIGN
        "Lcv%=:\n\t"
        "s_waitcnt lgkmcnt(6)\n\t"
        "v_mfma_f32_16x16x32_bf16 %0, v[240:243], v[228:231], %0\n\t"
        "ds_read_b128 v[252:255], %4 offset:192\n\t"
        "v_mfma_f32_16x16x32_bf16 %1, v[240:243], v[232:235], %1\n\t"
        "ds_read2_b32 v[220:221], %6 offset0:48 offset1:49\n\t"
        "v_mfma_f32_16x16x32_bf16 %2, v[240:243], v[236:239], %2\n\t"
        "ds_read2_b32 v[222:223], %6 offset0:50 offset1:51\n\t"
        "v_add_u32 %5, 0x140, %4\n\t"
        "v_mfma_f32_16x16x32_bf16 %3, v[240:243], v[208:211], %3\n\t"
        "v_min_u32 %5, %5, %9\n\t"
        "s_waitcnt lgkmcnt(6)\n\t"
        "v_mfma_f32_16x16x32_bf16 %0, v[244:247], v[232:235], %0\n\t"
        "ds_read_b128 v[240:243], %5 offset:0\n\t"
        "v_mfma_f32_16x16x32_bf16 %1, v[244:247], v[236:239], %1\n\t"
        "ds_read2_b32 v[224:225], %6 offset0:64 offset1:65\n\t"
        "v_mfma_f32_16x16x32_bf16 %2, v[244:247], v[208:211], %2\n\t"
        "ds_read2_b32 v[226:227], %6 offset0:66 offset1:67\n\t"
        "v_mfma_f32_16x16x32_bf16 %3, v[244:247], v[212:215], %3\n\t"
        "s_waitcnt lgkmcnt(6)\n\t"
        "v_mfma_f32_16x16x32_bf16 %0, v[248:251], v[236:239], %0\n\t"
        "ds_read_b128 v[244:247], %5 offset:64\n\t"
        "v_mfma_f32_16x16x32_bf16 %1, v[248:251], v[208:211], %1\n\t"
        "ds_read2_b32 v[228:229], %6 offset0:80 offset1:81\n\t"
        "v_mfma_f32_16x16x32_bf16 %2, v[248:251], v[212:215], %2\n\t"
        "ds_read2_b32 v[230:231], %6 offset0:82 offset1:83\n\t"
        "v_add_u32 %4, 0x140, %5\n\t"
        "v_mfma_f32_16x16x32_bf16 %3, v[248:251], v[216:219], %3\n\t"
        "v_min_u32 %4, %4, %9\n\t"
        "s_waitcnt lgkmcnt(6)\n\t"
        "v_mfma_f32_16x16x32_bf16 %0, v[252:255], v[208:211], %0\n\t"
        "ds_read_b128 v[248:251], %5 offset:128\n\t"
        "v_mfma_f32_16x16x32_bf16 %1, v[252:255], v[212:215], %1\n\t"
        "ds_read2_b32 v[232:233], %6 offset0:96 offset1:97\n\t"
        "v_mfma_f32_16x16x32_bf16 %2, v[252:255], v[216:219], %2\n\t"
        "ds_read2_b32 v[234:235], %6 offset0:98 offset1:99\n\t"
        "v_mfma_f32_16x16x32_bf16 %3, v[252:255], v[220:223], %3\n\t"
        "s_waitcnt lgkmcnt(6)\n\t"
        "v_mfma_f32_16x16x32_bf16 %0, v[240:243], v[212:215], %0\n\t"
        "ds_read_b128 v[252:255], %5 offset:192\n\t"
        "v_mfma_f32_16x16x32_bf16 %1, v[240:243], v[216:219], %1\n\t"
        "ds_read2_b32 v[236:237], %6 offset0:112 offset1:113\n\t"
        "v_mfma_f32_16x16x32_bf16 %2, v[240:243], v[220:223], %2\n\t"
        "ds_read2_b32 v[238:239], %6 offset0:114 offset1:115\n\t"
        "v_mfma_f32_16x16x32_bf16 %3, v[240:243], v[224:227], %3\n\t"
        "s_waitcnt lgkmcnt(6)\n\t"
        "v_mfma_f32_16x16x32_bf16 %0, v[244:247], v[216:219], %0\n\t"
        "ds_read_b128 v[240:243], %4 offset:0\n\t"
        "v_mfma_f32_16x16x32_bf16 %1, v[244:247], v[220:223], %1\n\t"
        "ds_read2_b32 v[208:209], %6 offset0:128 offset1:129\n\t"
        "v_mfma_f32_16x16x32_bf16 %2, v[244:247], v[224:227], %2\n\t"
        "ds_read2_b32 v[210:211], %6 offset0:130 offset1:131\n\t"
        "v_mfma_f32_16x16x32_bf16 %3, v[244:247], v[228:231], %3\n\t"
        "s_waitcnt lgkmcnt(6)\n\t"
        "v_mfma_f32_16x16x32_bf16 %0, v[248:251], v[220:223], %0\n\t"
        "ds_read_b128 v[244:247], %4 offset:64\n\t"
        "v_mfma_f32_16x16x32_bf16 %1, v[248:251], v[224:227], %1\n\t"
        "ds_read2_b32 v[212:213], %6 offset0:144 offset1:145\n\t"
        "v_mfma_f32_16x16x32_bf16 %2, v[248:251], v[228:231], %2\n\t"
        "ds_read2_b32 v[214:215], %6 offset0:146 offset1:147\n\t"
        "v_mfma_f32_16x16x32_bf16 %3, v[248:251], v[232:235], %3\n\t"
        "s_sub_u32 %7, %7, 1\n\t"
        "s_waitcnt lgkmcnt(6)\n\t"
        "v_mfma_f32_16x16x32_bf16 %0, v[252:255], v[224:227], %0\n\t"
        "ds_read_b128 v[248:251], %4 offset:128\n\t"
        "v_mfma_f32_16x16x32_bf16 %1, v[252:255], v[228:231], %1\n\t"
        "ds_read2_b32 v[216:217], %6 offset0:160 offset1:161\n\t"
        "v_mfma_f32_16x16x32_bf16 %2, v[252:255], v[232:235], %2\n\t"
        "ds_read2_b32 v[218:219], %6 offset0:162 offset1:163\n\t"
        "v_mfma_f32_16x16x32_bf16 %3, v[252:255], v[236:239], %3\n\t"
        "v_add_u32 %6, 0x200, %6\n\t"
        "s_cbranch_scc0 Lcv%=\n\t"
        "s_waitcnt lgkmcnt(0)\n\t"
        "s_nop 7\n\t"
        "s_nop 7\n\t"
        : "+v"(acc[0]), "+v"(acc[1]), "+v"(acc[2]), "+v"(acc[3]), "+v"(ua), "=&v"(un), "+v"(ha), "+s"(nit), "=&s"(cn)
        : "v"(uz), "v"(gate)
        : "memory", "scc", "vcc", "v208", "v209", "v210", "v211", "v212", "v213", "v214", "v215", "v216", "v217", "v218", "v219", "v220", "v221", "v222", "v223", "v224", "v225", "v226", "v227", "v228", "v229", "v230", "v231", "v232", "v233", "v234", "v235", "v236", "v237", "v238", "v239", "v240", "v241", "v242", "v243", "v244", "v245", "v246", "v247", "v248", "v249", "v250", "v251", "v252", "v253", "v254", "v255");
#pragma unroll
    for (int j = 0; j < 8; ++j)
        sts32(yst + (32 * ((j >> 1) ^ q) + 2 * a + 128 * (2 * q + (j & 1))) * 2, pk2(acc[j >> 1][2 * (j & 1)], acc[j >> 1][2 * (j & 1) + 1]));
    asm volatile("" ::: "memory");
}
DEV void y_out(rsrc_t rw, u32 yst, u32 ya, u32 vo, int lane) {
    const u32 o = (lane * 16) ^ ((lane >> 5) << 6);
    const u32x4 y0 = lds128u(yst + o), y1 = lds128u(yst + 1024 + (o ^ 128));
    st128(y0, vo, rw, ya);
    st128(y1, vo, rw, ya + 1024);
}

DEV void g2_ldw(bf16x8 (&wr)[5], rsrc_t rw, u32 vo, u32 wsrc, int ks) {
#pragma unroll
    for (int f = 0; f < 5; ++f) wr[f] = asbf(ld128c(rw, vo, wsrc + (f * 54 + ks) * 1024));
}
DEV void mm5_opt(f32x4 (&c)[5], const bf16x8 (&a)[5], bf16x8 h, int w) {
    asm volatile(
        "s_cmp_eq_u32 %11, 3\n\t"
        "s_cbranch_scc1 Lmo%=\n\t"
        "v_mfma_f32_16x16x32_bf16 %0, %5, %10, %0\n\t"
        "v_mfma_f32_16x16x32_bf16 %1, %6, %10, %1\n\t"
        "v_mfma_f32_16x16x32_bf16 %2, %7, %10, %2\n\t"
        "v_mfma_f32_16x16x32_bf16 %3, %8, %10, %3\n\t"
        "v_mfma_f32_16x16x32_bf16 %4, %9, %10, %4\n"
        "Lmo%=:"
        : "+v"(c[0]), "+v"(c[1]), "+v"(c[2]), "+v"(c[3]), "+v"(c[4]) : "v"(a[0]), "v"(a[1]), "v"(a[2]), "v"(a[3]), "v"(a[4]), "v"(h), "s"(w) : "scc");
}
DEV void g2_fin(rsrc_t rw, const bf16x8 (&wr)[6][5], const bf16x8 (&wx)[2][5], int w, int off, int blk, int lane) {
    f32x4 acc[4][5];
#pragma unroll
    for (int t = 0; t < 4; ++t)
#pragma unroll
        for (int f = 0; f < 5; ++f) acc[t][f] = f32x4{0.f, 0.f, 0.f, 0.f};
    bf16x8 hf[32];
    const u32 vz = w == 3 ? OOB : (u32)lane * 16;
    const u32 la = a_area(w) + lane * 16;
    auto ldh = [&](int n) { hf[n] = asbf(ld128c(rw, (n & 7) == 6 ? vz : (u32)lane * 16, WS_H + ((blk + (((n >> 3) + 1 + w) & 3)) * 54 + g2_step(off, w, n & 7)) * 1024)); };
#pragma unroll
    for (int n = 0; n < LKA; ++n) ldh(n);
    SB();
#pragma unroll
    for (int n = 0; n < 32; ++n) {
        const int t = n >> 3, i = n & 7;
        if (SKIP && i == 6) mm5_opt(acc[t], wx[0], hf[n], w);
        else {
#pragma unroll
            for (int f = 0; f < 5; ++f) acc[t][f] = MFMA16(i < 6 ? wr[i < 6 ? i : 0][f] : wx[i & 1][f], hf[n], acc[t][f]);
        }
        if (n + LKA < 32) ldh(n + LKA);
        if (t > 0 && i < 5) sts128f(la + ((t - 1) * 5 + i) * 1024, acc[t > 0 ? t - 1 : 0][i < 5 ? i : 0]);
        if (n == 29) lds_inc(L_AF);
        SB();
    }
    lds_spin(L_AS, 4);
#pragma unroll
    for (int f = 0; f < 5; ++f) sts128f(la + (15 + f) * 1024, acc[3][f]);
    ldsflagw(L_FREE + w * 4, 11u);
}
DEV void g2_pre(const Args& A, rsrc_t rw, const bf16x8 (&wr)[6][5], int w, int off, int blk, int cgp, u32 tag, u32 sv, u32 f2, u32 ya, int lane) {
    f32x4 acc[4][5];
#pragma unroll
    for (int t = 0; t < 4; ++t)
#pragma unroll
        for (int f = 0; f < 5; ++f) acc[t][f] = f32x4{0.f, 0.f, 0.f, 0.f};
    bf16x8 hf[24];
    u32x4 yv[3];
    u32x2 xv[5];
    const u32 yfa = WS_F2 + (cgp * 32 + (lane & 31)) * F2S, yo = (lane >> 3) * 4096 + (lane & 7) * 16;
    auto ldy = [&](u32 vo) {
#pragma unroll
        for (int j = 0; j < 3; ++j) yv[j] = ld128c(rw, (j == 2 && w >= 2) ? OOB : vo, WS_Y + (cgp * 80 + (j * 4 + w) * 8) * 4096 + blk * 32);
    };
    const u32 vz = w == 3 ? OOB : (u32)lane * 16;
    auto ldh = [&](int n) { hf[n] = asbf(ld128c(rw, n % 6 == 5 ? vz : (u32)lane * 16, WS_H + ((blk + ((n / 6 + 1 + w) & 3)) * 54 + g2_step(off, w + 4, n % 6)) * 1024)); };
    y_out(rw, YST + w * 2048, ya, f2 ? (u32)lane * 16 : OOB, lane);
    lds_inc(L_CD);
    SB();
#pragma unroll
    for (int n = 0; n < LKB; ++n) ldh(n);
    SB();
    asm volatile("s_waitcnt vmcnt(%0)" ::"n"(LKB) : "memory");
    st32(tag, (lds_add(f2 ? L_YCNT : L_NIL) == 3u && lane == 0 && f2) ? f2 : OOB, rw);
#pragma unroll
    for (int f = 0; f < 5; ++f) xv[f] = *(const AS3 u32x2*)(uintptr_t)(a_area(w) + 15 * 1024 + (lane & 15) * 160 + (lane >> 4) * 8 + f * 32);
    lds_inc(L_AS);
    SB();
#pragma unroll
    for (int n = 0; n < 24; ++n) {
        const int t = n / 6, i = n % 6;
        if (n == 6) lds_spin(L_CD, 4);
        if (SKIP && i == 5) mm5_opt(acc[t], wr[5], hf[n], w);
        else {
#pragma unroll
            for (int f = 0; f < 5; ++f) acc[t][f] = MFMA16(wr[i][f], hf[n], acc[t][f]);
        }
        if (n + LKB < 24) ldh(n + LKB);
        if (t > 0 && i < 5) sts128f(RB + ((((t + w) & 3) * 3 + t - 1) * 5 + i) * 1024 + lane * 16, acc[t > 0 ? t - 1 : 0][i < 5 ? i : 0]);
        SB();
    }
    asm volatile("s_nop 7\n\ts_nop 7" ::: "memory");
    lds_inc(L_AD);
    if (w == 2) { g_poll1(rw, yfa, tag); ldsflagw(L_YOK, 1u); }
    else lds_spin(L_YOK, 1);
    ldy(yo);
    SB();
    f32x4 xn[5], pr[15];
    const u32 ta = RB + w * 15360;
    lds_spin(L_AD, 4);
#pragma unroll
    for (int j = 0; j < 15; ++j) pr[j] = lds128f(ta + j * 1024 + lane * 16);
    {
        const float r = __builtin_amdgcn_rcpf(bhi(sv)), mr = -blo(sv) * r;
#pragma unroll
        for (int f = 0; f < 5; ++f) {
            const f32x4 xx = {blo(xv[f][0]), bhi(xv[f][0]), blo(xv[f][1]), bhi(xv[f][1])};
            acc[3][f] += xx;
            xn[f] = xx * r + mr;
        }
    }
#pragma unroll
    for (int f = 0; f < 5; ++f) acc[3][f] = (acc[3][f] + pr[f]) + (pr[5 + f] + pr[10 + f]);
    SB();
    lds_spin(L_AF, 4);
#pragma unroll
    for (int j = 0; j < 15; ++j) pr[j] = lds128f(a_area((w + 3 - j / 5) & 3) + j * 1024 + lane * 16);
#pragma unroll
    for (int f = 0; f < 5; ++f) acc[3][f] = (acc[3][f] + pr[f]) + (pr[5 + f] + pr[10 + f]);
    SB();
#pragma unroll
    for (int j = 0; j < 3; ++j) if (j < 2 || w < 2) sts128(YT + (j * 4 + w) * 1024 + lane * 16, yv[j]);
    lds_inc(L_YRDY);
    lds_spin(L_YRDY, 4);
#pragma unroll
    for (int f = 0; f < 5; ++f) {
        const u32x2 y = ldstru(YT + (16 * f + (lane >> 2)) * 128 + w * 32 + (lane & 3) * 8);
        acc[3][f] += xn[f] * f32x4{blo(y[0]), bhi(y[0]), blo(y[1]), bhi(y[1])};
    }
    SB();
    lds_spin(L_FREE + w * 4, 11);
#pragma unroll
    for (int f = 0; f < 5; ++f) pr[f] = lds128f(a_area(w) + (15 + f) * 1024 + lane * 16);
#pragma unroll
    for (int f = 0; f < 5; ++f) {
        const f32x4 o = acc[3][f] + pr[f];
        *(AS3 u32x2*)(uintptr_t)(ta + (lane & 15) * 160 + (lane >> 4) * 8 + f * 32) = u32x2{pk2(o[0], o[1]), pk2(o[2], o[3])};
    }
    asm volatile("" ::: "memory");
    rsrc_t ro = mkrs(A.out, S * D * 2);
#pragma unroll
    for (int j = 0; j < 3; ++j) {
        const int e = j * 64 + lane, r = e / 10;
        __builtin_amdgcn_raw_buffer_store_b128(lds128u(ta + e * 16), ro, e < 160 ? ((blk + w) * 16 + r) * (D * 2) + cgp * 160 + (e - r * 10) * 16 : OOB, 0, OUTAUX);
    }
}

#define P0A 0
#define WPRE 15
#define WPRE2 30
#define IDLET 880u
#define THRB 99
#define POLL7 6
#define POLLAT 6
__global__ __launch_bounds__(512, 2) void fused_kernel(Args A) {
    const int b = blockIdx.x, lane = threadIdx.x & 63, wid = rfl(threadIdx.x >> 6);
    const int xcd = b & 7, idx = b >> 3;
    rsrc_t rw = mkrs(A.ws, WS_SIZE);
    const int hx = (xcd + 2) >> 3, n4 = xcd * 4, dv = (0x45333333u >> n4) & 15, c = ((0x540u >> xcd) & 32) + idx;
    const int cq = (c * ((0x344056u >> (dv * 8 - 24)) & 255)) >> 8, cr = c - cq * dv, over = c - dv * (21 - 15 * hx);
    const int rt = cr + ((0x50663300u >> n4) & 15), grp = cq + 21 * hx;
    const int idle = (over + ((0x53221100u >> n4) & 15)) | (over >> 31);
    const int tr = cq * (1 + hx) + cr * (3 - hx) + ((0xB0221100u >> n4) & 15);
    int ro = tr - 10 * ((tr * 26) >> 8);
    const bool busy = idle < 0;
    if (threadIdx.x >= 448 && lane < 36) ldsflagw(lane < 32 ? FLG + lane * 4 : ONES + (lane - 32) * 4, lane >= 32 ? 0x3F803F80u : (!busy && lane < 8) ? 10u : 0u);
    G1Src g;
    g.rx = mkrs(A.x, S * D * 2); g.rg = mkrs(A.wg, D * DF * 2); g.ru = mkrs(A.wu, D * DF * 2); g.ro = ro;
    { int row = lane >> 3;
      g.xv0 = (rt * RT + row) * (D * 2) + (((lane & 7) ^ (row >> 1)) << 4); g.xv1 = g.xv0 ^ 64; g.xl = row < 4 ? g.xv0 : OOB;
      g.wv0 = row * (DF * 2) + grp * 128 + (((lane & 7) ^ (((row >> 1) & 1) << 1)) << 4); g.wv1 = g.wv0 ^ 64; }
    if (wid < 4) {
        PRIO(3);
        if (busy) {
            if (wid & 1) { g.xv0 = g.xv1; g.wv0 = g.wv1; g.xl = OOB; }
            const u32 ks = ro * 128 + (wid & 1) * 8 * D * 2, kw = ro * (64 * DF * 2) + (wid & 1) * 8 * DF * 2, lo = (wid & 1) * 1024;
            if (wid < 2) {
#pragma unroll
                for (int p = 0; p < 22; p += 2) dma16(XS + lo + p * 1024, g.xv0, g.rx, ks + p * 8 * D * 2);
            } else {
#pragma unroll
                for (int p = 0; p < 16; p += 2) dma16(WO + 2 * WSZ + lo + p * 1024, g.wv0, (p >> 3) ? g.ru : g.rg, kw + (p & 7) * 8 * DF * 2);
#pragma unroll
                for (int p = 22; p < 28; p += 2) dma16(XS + lo + p * 1024, g.xv0, g.rx, ks + p * 8 * D * 2);
                if (wid == 2) dma16(XS + 28 * 1024, g.xl, g.rx, ks + 28 * 8 * D * 2);
            }
        }
    }
    u32 cpf = 0, ep = 0;
        if (wid == 7) {
            uint64_t pc; u32 len;
            asm volatile("s_getpc_b64 %0\nLcs%=:\n\ts_mov_b32 %1, Lce%=-Lcs%=\n\t.subsection 2\nLce%=:\n\t.subsection 0" : "=s"(pc), "=s"(len));
            cpf = __builtin_amdgcn_raw_buffer_load_b32(mkrs((const void*)pc, min(len, 40960u)), lane < 10 ? (idx * 10 + lane) * 128 : OOB, 0, 0);
        }
        if (wid == 6) ep = __builtin_amdgcn_raw_buffer_load_b32(rw, 0, 0, 16);
    BARL();
    const u32 ts0 = (u32)__builtin_amdgcn_s_memrealtime();
    if (busy && wid < 4) { PRIO(1); WAITV(0);
        lds_inc(L_CNT0); }
    const int pr = (b >> 4) * 8 + xcd, mem = idx & 1;
    const int c0 = pr * 5 + mem * 3;
    u32x4 e[12];
    const u32 fr = L_FREE + (lane & 3) * 4;
    if (!busy || wid >= 6) {
        const int q = rt * 27 + grp;
        int kvt = 320, hf = 0, nkv = 1, wdp = -1, stu = -1;
        int h0 = 0, h1 = 0;
        if (busy) { kvt = q; hf = wid - 6; wdp = q; h0 = 1 - hf; h1 = 2 - hf; stu = q + q / 18; }
        else {
            if (wid < 4) { kvt = 243 + idle * 6 + wid; nkv = 2; }
            else { kvt = 245 + idle * 6 + (wid >> 1); hf = wid & 1; wdp = 241 + 2 * idle + (wid >> 1); h0 = hf; h1 = hf + 1; }
            stu = 19 * idle + 18;
            if (kvt == 320) { nkv = 0; wdp = 268 + hf; h0 = 0; h1 = 2; }
        }
        const int r32 = (kvt / 10) * 2 + hf, cg = kvt % 10;
        const u32 lb = busy ? STG : wid * 8192;
        u32 sx = 0;
        if (P0A > 0) lds_spin(fr, P0A);
        if (!busy && wid < 4) lds_spin(L_ISS0, 4);
        wd_issue(A, wdp, h0, h1, lb, lane);
        if (wid == 6) {
#pragma unroll
            for (int i = 0; i < 10; ++i) e[i] = ld128(g.rx, (stu * 8 + ((lane >> 2) & 3)) * (D * 2) + (lane >> 4) * 64 + (lane & 3) * 16, (i / 5) * 4 * D * 2 + (i % 5) * 256);
        }
        KvRegs R, R2;
        u32x4 w[2];
        { rsrc_t rs = mkrs(A.sk, D * 8); w[0] = ld128(rs, cg * 512 + (lane & 15) * 32, 0); w[1] = ld128(rs, cg * 512 + (lane & 15) * 32, 16); }
        kv_issue(A, R, r32, cg, lane);
        if (nkv == 2) kv_issue(A, R2, r32 + 1, cg, lane);
        SB();
        if (!busy && wid >= 4) lds_inc(L_ISS0);
        PRIO(P0PRIO);
        if (wid == 6) {
            ep += 0x9E3779B1u;
            ldsflagw(L_TAG, ep);
            ldsflagw(L_TAGOK, 1u);
            sx = row_stats(e, lane);
        }
        asm volatile("" ::"v"(R.vv[7]), "v"(R2.vv[7]), "v"(sx));
        if (wid == 6) st64(u32x2{sx, ep}, (lane & 14) == (lane >> 4) * 4 ? WS_ST + (stu * 8 + (lane & 1) * 4 + (lane >> 4)) * 8 : OOB, rw, 0);
#pragma unroll 1
        for (int r = 0; r < nkv; ++r) {
            kv_finish(R, w, rw, r32 + r, cg, lane);
            R = R2;
        }
        wd_finish(rw, wdp, h0, h1, lb, lane);
        asm volatile("" ::"v"(cpf));
        WAITV(0);
        lds_spin(L_TAGOK, 1);
        if (lds_add(wid < 4 ? L_PCNT2 : L_PCNT) == (busy ? 1u : 3u) && lane == 0) st32(ldsflag1(L_TAG), WS_FP + (wid < 4 ? 256 + idle : b) * 16, rw);
        if (wid == 7) {
            rsrc_t rh = mkrs(A.hk, D * S * 2);
#pragma unroll
            for (int i = 0; i < 12; ++i) e[i] = ld128n(rh, lane * 16, (i < 8 ? c0 + (i >> 2) : pr * 5 + 2) * 4096 + (i & 3) * 1024);
        }
        PRIO(0);
    }
    const int cgp = (xcd & 1) * 4 + (idx & 3), jr = idx >> 2, blk = ((xcd >> 1) * 8 + jr) * 4;
    const int off = wrapn(13 * (idx & 3) + 4 * (jr & 1) + 16 * (jr >> 1), 54);
    const u32 wsrc = WS_WD + cgp * 5 * 54 * 1024;
    const int w4 = wid & 3;
    bf16x8 wr[6][5];
    if (wid < 4) {
        g1_compute(busy, wr, wsrc, off, wid, lane, rw, rt, grp);
        WAITV(0);
        const u32 tag = rfl(ldsflag1(L_TAG));
        if (lds_add(L_HCNT) == 3u && lane == 0 && busy) st32(tag, WS_F1 + (rt * 32 + grp) * 128, rw);
        g2_ldw6<99>(wr, rw, wsrc, off, wid, lane, WG0 + 6 * WGS, 30);
        bf16x8 wx[2][5];
        g2_ldw(wx[0], rw, wid == 3 ? OOB : (u32)lane * 16, wsrc, g2_step(off, wid, 6));
        g2_ldw(wx[1], rw, wid == 3 ? OOB : (u32)lane * 16, wsrc, g2_step(off, wid, 7));
        SB();
        lds_spin(fr, 10);
        if (wid == PW) {
            const int r = (blk * 16 + (lane < 32 ? 0 : 63)) / RT;
            g_poll1(rw, WS_F1 + (r * 32 + min(lane & 31, 26)) * 128, tag);
            ldsflagw(L_HRDY, 1u);
            g2_ldw(wx[1], rw, lane * 16, wsrc, g2_step(off, wid, 7));
        } else lds_spin<SLH>(L_HRDY, 1);
        PRIO(0);
        g2_fin(rw, wr, wx, wid, off, blk, lane);
    } else {
        if (wid < 6) { if (busy) { PRIO(2); g1_loader(wid - 4, g, lane, rw); PRIO(0); } }
        u32x4 v[4];
        if (wid >= 6) {
            const u32 tg = rfl(ldsflag1(L_TAG));
            lds_spin<SLL>(fr, wid == 7 ? POLL7 : POLLAT);
            if (wid == 7) {
                if (busy) { st_gather(rw, rt * RT, tg, lane); ldsflagw(L_EPI, 2u); }
                lds_spin<SLP>(L_P0ALL, 1);
            } else {
                const u32 o = WS_FP + lane * 16;
                if (!busy) {
#pragma unroll 1
                    for (int i = 0; i < 64 && (u32)__builtin_amdgcn_s_memrealtime() - ts0 < IDLET; ++i) __builtin_amdgcn_s_sleep(8);
                }
                g_poll4(rw, o, o + 1024, o + 2048, o + 3072, tg);
                ldsflagw(L_P0ALL, 2u);
                if (idle == 0 && lane == 0) st32(tg, 0, rw);
            }
            lds_spin<SLH>(L_LDONE, 2);
        } else lds_spin<SLP>(L_P0ALL, 1);
        if (wid >= 6) PRIO(3); else PRIO(BPL);
        const u32 tag = rfl(ldsflag1(L_TAG));
        const u32 dvv = __builtin_amdgcn_raw_buffer_load_b16(mkrs(A.dv, D * 2), (pr * 5 + lane) * 2, 0, 0);
        const u32 so = WS_ST + ((blk + w4) * 16 + (lane & 15)) * 8;
        u32x2 sv = __builtin_amdgcn_raw_buffer_load_b64(rw, so, 0, 16);
        g2_ldw6<THRB>(wr, rw, wsrc, off, wid, lane, 0, WPRE);
        SB();
        if (wid >= 6) {
            if (wid == 6) {
                g_poll1(rw, WS_FP + (256 + min(lane, 12)) * 16, tag);
                ldsflagw(L_KVISS, 1u);
#pragma unroll
                for (int i = 0; i < 4; ++i) e[i] = ld128c(rw, WS_KV + lane * 16, (c0 + 1) * 4096 + i * 1024);
            } else lds_spin<SLP>(L_KVISS, 1);
#pragma unroll
            for (int i = 0; i < 4; ++i) v[i] = ld128c(rw, WS_KV + lane * 16, (wid == 6 ? c0 : pr * 5 + 2) * 4096 + i * 1024);
        }
        SB();
        g2_ldw6<99>(wr, rw, wsrc, off, wid, lane, WPRE, WPRE2);
        SB();
        if (wid >= 6) {
            lds_spin<SLP>(fr, 8);
            if (wid == 7) {
#pragma unroll
                for (int i = 0; i < 12; ++i) sts128(IMG + (i >> 2) * IMGSZ + 64 + (i & 3) * 1024 + lane * 16, e[i]);
            } else { const u32x4 e4[4] = {e[0], e[1], e[2], e[3]}; conv_build(e4, IMG + IMGSZ, lane); }
            conv_build(v, IMG + (wid == 6 ? 0 : 2 * IMGSZ), lane);
            lds_inc(L_IMG);
        }
        SB();
        g2_ldw6<99>(wr, rw, wsrc, off, wid, lane, WPRE2, 30);
        SB();
        PRIO(BPM);
        lds_spin<SLI>(L_IMG, 2);
        lds_spin(fr, 10);
        {
            rsrc_t rx = mkrs(A.x, S * D * 2);
#pragma unroll
            for (int j = 0; j < 3; ++j) {
                const int el = j * 64 + lane, r = el / 10;
                dma16(a_area(w4) + (15 + j) * 1024, el < 160 ? ((blk + w4) * 16 + r) * (D * 2) + cgp * 160 + (el - r * 10) * 16 : OOB, rx, 0);
            }
        }
        const u32 yst = YST + w4 * 2048;
        const int nt = wid == 7 ? 2 : 1;
        u32 ya = 0;
#pragma unroll 1
        for (int sl = 0; sl < nt; ++sl) {
            const int im = wid == 7 ? sl : w4;
            const int ci = im == 2 ? 2 : mem * 3 + im, cg = (wid == 7 || (mem && w4 == 2)) ? 0 : 1;
            if (sl) y_out(rw, yst, ya, lane * 16, lane);
            ya = WS_Y + (pr * 5 + ci) * 4096 + cg * 2048;
            conv_task(cg, blo(__builtin_amdgcn_readlane(dvv, ci)), IMG + im * IMGSZ, yst, L_GD + w4, lane);
        }
        *(volatile AS3 unsigned char*)(uintptr_t)(L_BD + w4) = 1;
        const bool late = rfl(ldsflag1(L_HRDY)) != 0;
        const u32 f2 = WS_F2 + (pr * 2 + mem) * F2S;
        if (!late) {
            y_out(rw, yst, ya, lane * 16, lane);
            WAITV(0);
            if (lds_add(L_YCNT) == 3u && lane == 0) st32(tag, f2, rw);
            lds_spin<SLP>(L_HRDY, 1);
        }
        PRIO(1);
#pragma unroll 1
        for (int it = 0; it < 0x4000 && __builtin_amdgcn_ballot_w64(sv[1] != tag) != 0; ++it) { asm volatile("" ::: "memory"); sv = __builtin_amdgcn_raw_buffer_load_b64(rw, so, 0, 16); }
        g2_pre(A, rw, wr, w4, off, blk, cgp, tag, sv[0], late ? f2 : 0u, ya, lane);
    }
}

extern "C" void kernel_launch(void* const* d_in, const int* in_sizes, int n_in, void* d_out, int out_size, void* d_ws, size_t ws_size, hipStream_t stream) {
    Args A;
    A.x = (const u16*)d_in[0]; A.k = (const u16*)d_in[1]; A.v = (const u16*)d_in[2]; A.sk = (const u16*)d_in[3]; A.hk = (const u16*)d_in[4];
    A.dv = (const u16*)d_in[5]; A.wg = (const u16*)d_in[7]; A.wu = (const u16*)d_in[8]; A.wd = (const u16*)d_in[9];
    A.out = (u16*)d_out; A.ws = (char*)d_ws;
    (void)hipFuncSetAttribute((const void*)fused_kernel, hipFuncAttributeMaxDynamicSharedMemorySize, LDS_BYTES);
    fused_kernel<<<256, 512, LDS_BYTES, stream>>>(A);
}
```
